```python
import torch

def _softmax(x, axis=-1):
    return torch.softmax(x, dim=axis)
def _gelu(x):
    return torch.nn.functional.gelu(x, approximate="tanh")
def _silu(x):
    return torch.nn.functional.silu(x)
def _sigmoid(x):
    return torch.sigmoid(x)
def _layernorm(x, axis=-1, eps=1e-5):
    m = x.mean(dim=axis, keepdim=True)
    v = x.var(dim=axis, keepdim=True, unbiased=False)
    return (x - m) / torch.sqrt(v + eps)
def _scatter_add(tgt, idx, src, axis=0):
    return tgt.clone().index_add_(axis, idx, src)
def _segment_sum(src, seg, n):
    out = torch.zeros((n,) + src.shape[1:], dtype=src.dtype)
    return out.index_add_(0, seg, src)
def _pad(x, pw):
    flat = tuple(v for pair in reversed(pw) for v in pair)
    return torch.nn.functional.pad(x, flat)

kernel_name = 'parallel_sum__sliding_block__glu_gated_bilinear__prompt'

def setup_inputs():

    _w = (lambda a: (lambda t: t.to(torch.bfloat16) if t.is_floating_point() else t)(torch.as_tensor(a)))
    def _A_setup():
        B, S, D = 1, 2048, 640
        return {
            "x": _w((lambda *s: torch.randn(*s, dtype=torch.bfloat16) * 0.05)(B, S, D)),
            "wqkv": _w((lambda *s: torch.randn(*s, dtype=torch.bfloat16) * 0.05)(D, 3 * D)),
            "wo": _w((lambda *s: torch.randn(*s, dtype=torch.bfloat16) * 0.05)(D, D)),
        }
    _A_in = _A_setup()

    def _B_setup():
        B, S, D, F = 1, 2048, 640, 1728
        return {
            "x": _w((lambda *s: torch.randn(*s, dtype=torch.bfloat16) * 0.05)(B, S, D)),
            "w_gate": _w((lambda *s: torch.randn(*s, dtype=torch.bfloat16) * 0.8)(D, F) * (D ** -0.5)),
            "w_up": _w((lambda *s: torch.randn(*s, dtype=torch.bfloat16) * 0.8)(D, F) * (D ** -0.5)),
            "w_down": _w((lambda *s: torch.randn(*s, dtype=torch.bfloat16) * 0.8)(F, D) * (F ** -0.5)),
        }
    _B_in = _B_setup()

    return {
        "A_x": _A_in["x"],
        "A_wqkv": _A_in["wqkv"],
        "A_wo": _A_in["wo"],
        "B_x": _B_in["x"],
        "B_w_gate": _B_in["w_gate"],
        "B_w_up": _B_in["w_up"],
        "B_w_down": _B_in["w_down"],
    }

def reference(A_x, A_wqkv, A_wo, B_x, B_w_gate, B_w_up, B_w_down):
    def _A_ref(x, wqkv, wo):
        B, S, D = x.shape
        H = 32; Dh = D // H; W = 256
        qkv = (lambda x, *d: x.permute(*d))((x @ wqkv).reshape(B, S, 3, H, Dh), 2, 0, 3, 1, 4)
        q, k, v = qkv[0], qkv[1], qkv[2]
        scores = torch.einsum("bhqd,bhkd->bhqk", q, k) * (Dh ** -0.5)
        idx = torch.arange(S)
        mask = (idx[:, None] - idx[None, :] >= 0) & (idx[:, None] - idx[None, :] < W)
        scores = torch.where(mask[None, None], scores, -1e9)
        p = _softmax(scores, axis=-1)
        o = torch.einsum("bhqk,bhkd->bhqd", p, v)
        return (lambda x, *d: x.permute(*d))(o, 0, 2, 1, 3).reshape(B, S, D) @ wo

    def _B_ref(x, w_gate, w_up, w_down):
        g = torch.einsum("bsd,df->bsf", x, w_gate)
        u = torch.einsum("bsd,df->bsf", x, w_up)
        h = g * u
        return torch.einsum("bsf,fd->bsd", h, w_down)

    x = A_x
    xn = _layernorm(x, axis=-1)
    return x + _A_ref(xn, A_wqkv, A_wo) + _B_ref(xn, B_w_gate, B_w_up, B_w_down)
```

```cpp
#include <hip/hip_runtime.h>
#include <hip/hip_bf16.h>
typedef unsigned short u16;
__device__ __forceinline__ float bf2f(u16 b){ return __uint_as_float(((unsigned)b)<<16); }
__device__ __forceinline__ u16 f2bf(float f){ unsigned u=__float_as_uint(f); u += 0x7FFFu + ((u>>16)&1u); return (u16)(u>>16); }
constexpr int S=2048, D=640, H=32, DH=20, W=256, F=1728;

__global__ void ln_k(const u16* x, u16* xn){
  int row=blockIdx.x; int t=threadIdx.x;
  float v[10]; float s=0;
  for(int i=0;i<10;i++){ v[i]=bf2f(x[row*D+t+64*i]); s+=v[i]; }
  for(int o=32;o>=1;o>>=1) s+=__shfl_xor(s,o);
  float m=s/D; float q=0;
  for(int i=0;i<10;i++){ float d=v[i]-m; q+=d*d; }
  for(int o=32;o>=1;o>>=1) q+=__shfl_xor(q,o);
  float rs=rsqrtf(q/D+1e-5f);
  for(int i=0;i<10;i++) xn[row*D+t+64*i]=f2bf((v[i]-m)*rs);
}
__global__ void gemm_k(const u16* A,const u16* B,u16* C,int M,int N,int K){
  int n=blockIdx.x*blockDim.x+threadIdx.x; int m=blockIdx.y;
  if(n>=N) return;
  float acc=0;
  for(int k=0;k<K;k++) acc+=bf2f(A[m*K+k])*bf2f(B[k*N+n]);
  C[m*N+n]=f2bf(acc);
}
__global__ void attn_k(const u16* qkv,u16* o){
  int q=blockIdx.x, h=blockIdx.y; int t=threadIdx.x;
  __shared__ float red[256];
  __shared__ float p[256];
  int key=q-t; float s=-1e30f;
  if(key>=0){ s=0; for(int d=0;d<DH;d++) s+=bf2f(qkv[q*3*D+h*DH+d])*bf2f(qkv[key*3*D+D+h*DH+d]); s*=0.22360679775f; }
  red[t]=s; __syncthreads();
  for(int o2=128;o2>=1;o2>>=1){ if(t<o2) red[t]=fmaxf(red[t],red[t+o2]); __syncthreads(); }
  float mx=red[0]; __syncthreads();
  float e=(key>=0)?__expf(s-mx):0.f; p[t]=e; red[t]=e; __syncthreads();
  for(int o2=128;o2>=1;o2>>=1){ if(t<o2) red[t]+=red[t+o2]; __syncthreads(); }
  float l=red[0];
  if(t<DH){ float acc=0; for(int j=0;j<256;j++){ int kk=q-j; if(kk>=0) acc+=p[j]*bf2f(qkv[kk*3*D+2*D+h*DH+t]); } o[q*D+h*DH+t]=f2bf(acc/l); }
}
__global__ void mul_k(const u16* g,const u16* u,u16* h,int n){ int i=blockIdx.x*blockDim.x+threadIdx.x; if(i<n) h[i]=f2bf(bf2f(g[i])*bf2f(u[i])); }
__global__ void fin_k(const u16* x,const u16* a,const u16* b,u16* out,int n){ int i=blockIdx.x*blockDim.x+threadIdx.x; if(i<n) out[i]=f2bf(bf2f(x[i])+bf2f(a[i])+bf2f(b[i])); }

extern "C" void kernel_launch(void* const* d_in, const int* in_sizes, int n_in, void* d_out, int out_size, void* d_ws, size_t ws_size, hipStream_t stream) {
  const u16* x=(const u16*)d_in[0]; const u16* wqkv=(const u16*)d_in[1]; const u16* wo=(const u16*)d_in[2];
  const u16* wg=(const u16*)d_in[4]; const u16* wu=(const u16*)d_in[5]; const u16* wd=(const u16*)d_in[6];
  u16* ws=(u16*)d_ws;
  u16* xn=ws; u16* qkv=xn+S*D; u16* o=qkv+S*3*D; u16* a=o+S*D; u16* g=a+S*D; u16* u=g+S*F; u16* h=u+S*F; u16* b=h+S*F;
  ln_k<<<S,64,0,stream>>>(x,xn);
  gemm_k<<<dim3((3*D+255)/256,S),256,0,stream>>>(xn,wqkv,qkv,S,3*D,D);
  attn_k<<<dim3(S,H),256,0,stream>>>(qkv,o);
  gemm_k<<<dim3((D+255)/256,S),256,0,stream>>>(o,wo,a,S,D,D);
  gemm_k<<<dim3((F+255)/256,S),256,0,stream>>>(xn,wg,g,S,F,D);
  gemm_k<<<dim3((F+255)/256,S),256,0,stream>>>(xn,wu,u,S,F,D);
  mul_k<<<(S*F+255)/256,256,0,stream>>>(g,u,h,S*F);
  gemm_k<<<dim3((D+255)/256,S),256,0,stream>>>(h,wd,b,S,D,F);
  fin_k<<<(S*D+255)/256,256,0,stream>>>(x,a,b,(u16*)d_out,S*D);
}
```

```cpp
#include <hip/hip_runtime.h>
#include <hip/hip_bf16.h>
#include <stdint.h>

typedef unsigned short u16;
typedef short bf16x8 __attribute__((ext_vector_type(8)));
typedef short s16x4 __attribute__((ext_vector_type(4)));
typedef float f32x16 __attribute__((ext_vector_type(16)));
typedef float f32x4 __attribute__((ext_vector_type(4)));
typedef float f32x2 __attribute__((ext_vector_type(2)));
typedef unsigned u32x4 __attribute__((ext_vector_type(4)));
typedef unsigned u32x2 __attribute__((ext_vector_type(2)));
typedef __bf16 bf16x2_t __attribute__((ext_vector_type(2)));
#define LAS __attribute__((address_space(3)))
#define DI __device__ __forceinline__
typedef LAS const char* lds_cptr;
typedef short v4i16_t __attribute__((ext_vector_type(4)));

constexpr int S_ = 2048, D_ = 640, NH = 32, DH = 20, F_ = 1728;
constexpr int NKS3 = 74;
constexpr size_t WS_XT = 0;
constexpr size_t WS_QKV = WS_XT + (size_t)64 * 40 * 1024;
constexpr size_t WS_ACT = WS_QKV + (size_t)480 * 16384;
constexpr size_t WS_WT3 = WS_ACT + (size_t)128 * NKS3 * 1024;
constexpr size_t WS_END = WS_WT3 + (size_t)8 * NKS3 * 5 * 1024;

DI float bf2f(u16 b) { return __uint_as_float(((unsigned)b) << 16); }
DI unsigned pk2(float a, float b) { f32x2 v = {a, b}; bf16x2_t r = __builtin_convertvector(v, bf16x2_t); return __builtin_bit_cast(unsigned, r); }
DI s16x4 vtr(lds_cptr p) { return __builtin_bit_cast(s16x4, __builtin_amdgcn_ds_read_tr16_b64_v4i16((LAS v4i16_t*)p)); }
DI bf16x8 cat8(s16x4 a, s16x4 b) { return __builtin_shufflevector(a, b, 0, 1, 2, 3, 4, 5, 6, 7); }
DI void glds16(unsigned voff, const void* sbase, unsigned dst) {
    unsigned sv;
    asm volatile("s_mov_b32 %0, m0\n\ts_mov_b32 m0, %3\n\ts_nop 0\n\tglobal_load_lds_dwordx4 %1, %2\n\ts_mov_b32 m0, %0"
                 : "=&s"(sv) : "v"(voff), "s"(sbase), "s"(dst) : "memory");
}
DI const char* uptr(const char* p) {
    const uint64_t v = (uint64_t)p;
    const unsigned lo = __builtin_amdgcn_readfirstlane((unsigned)v), hi = __builtin_amdgcn_readfirstlane((unsigned)(v >> 32));
    return (const char*)(((uint64_t)hi << 32) | lo);
}
#define WAIT_VM(n) asm volatile("s_waitcnt vmcnt(%0)" ::"n"(n) : "memory")
#define WAIT_LGKM0() asm volatile("s_waitcnt lgkmcnt(0)" ::: "memory")
#define BAR() do { asm volatile("" ::: "memory"); __builtin_amdgcn_s_barrier(); asm volatile("" ::: "memory"); } while (0)
#define SBAR() __builtin_amdgcn_sched_barrier(0)
#define MFMA32(a, b, c) __builtin_amdgcn_mfma_f32_32x32x16_bf16((a), (b), (c), 0, 0, 0)
#define MFMA16(a, b, c) __builtin_amdgcn_mfma_f32_16x16x32_bf16((a), (b), (c), 0, 0, 0)

__global__ __launch_bounds__(256) void prep_k(const u16* __restrict__ x, const u16* __restrict__ wo, const u16* __restrict__ wd, char* __restrict__ ws) {
    __shared__ __attribute__((aligned(16))) u16 sm[32 * 328];
    const int tid = threadIdx.x, lane = tid & 63, wid = tid >> 6;
    if (blockIdx.x < 256) {
        const int t0 = blockIdx.x * 8;
#pragma unroll
        for (int r = 0; r < 2; ++r) {
            const int tok = t0 + wid * 2 + r;
            const unsigned* row = (const unsigned*)(x + (size_t)tok * D_);
            float v[10]; float s = 0.f;
#pragma unroll
            for (int i = 0; i < 5; ++i) { unsigned w = row[lane + 64 * i]; v[2 * i] = __uint_as_float(w << 16); v[2 * i + 1] = __uint_as_float(w & 0xffff0000u); s += v[2 * i] + v[2 * i + 1]; }
#pragma unroll
            for (int o = 32; o >= 1; o >>= 1) s += __shfl_xor(s, o);
            const float m = s * (1.f / D_); float q = 0.f;
#pragma unroll
            for (int i = 0; i < 10; ++i) { const float d = v[i] - m; q += d * d; }
#pragma unroll
            for (int o = 32; o >= 1; o >>= 1) q += __shfl_xor(q, o);
            const float rs = rsqrtf(q * (1.f / D_) + 1e-5f);
#pragma unroll
            for (int i = 0; i < 5; ++i) ((unsigned*)sm)[(wid * 2 + r) * 324 + lane + 64 * i] = pk2((v[2 * i] - m) * rs, (v[2 * i + 1] - m) * rs);
        }
        __syncthreads();
        char* xt = ws + WS_XT + (size_t)(t0 >> 5) * 40 * 1024;
        for (int i = tid; i < 640; i += 256) {
            const int tk = i & 7, c = i >> 3;
            const u32x4 v = *(const u32x4*)((const char*)sm + tk * 1296 + c * 16);
            *(u32x4*)(xt + (size_t)(c >> 1) * 1024 + ((c & 1) * 32 + (t0 & 31) + tk) * 16) = v;
        }
    } else {
        const int u = blockIdx.x - 256;
        const int ks = u >> 1, ch = u & 1;
        for (int i = tid; i < 32 * 40; i += 256) {
            const int sr = i / 40, c8 = i % 40;
            const int kg = sr >> 3, j = sr & 7;
            const u16* src;
            if (ks < 20) {
                const int slot = 4 * ks + kg, a = slot / 5, ii = slot % 5;
                int head, d;
                if (ii < 4) { head = 2 * a + (ii >> 1); d = (ii & 1) * 4 + (j & 3) + 8 * (j >> 2); }
                else { head = 2 * a + (j >> 2); d = 16 + (j & 3); }
                src = wo + (size_t)(head * DH + d) * D_;
            } else {
                const int k = 32 * (ks - 20) + 16 * (kg >> 1) + 8 * (j >> 2) + 4 * (kg & 1) + (j & 3);
                src = wd + (size_t)k * D_;
            }
            *(u32x4*)(sm + sr * 328 + c8 * 8) = *(const u32x4*)(src + ch * 320 + c8 * 8);
        }
        __syncthreads();
        for (int i = tid; i < 4 * 5 * 64; i += 256) {
            const int l = i & 63, ng = (i >> 6) % 5, ntl = (i >> 6) / 5;
            const int n = l & 15, kg = l >> 4;
            const int col = ntl * 80 + 20 * (n >> 2) + 4 * ng + (n & 3);
            unsigned w[4];
#pragma unroll
            for (int j = 0; j < 4; ++j) w[j] = (unsigned)sm[(kg * 8 + 2 * j) * 328 + col] | ((unsigned)sm[(kg * 8 + 2 * j + 1) * 328 + col] << 16);
            const int nt = ch * 4 + ntl;
            *(u32x4*)(ws + WS_WT3 + ((size_t)(nt * NKS3 + ks) * 5 + ng) * 1024 + l * 16) = (u32x4){w[0], w[1], w[2], w[3]};
        }
    }
}

constexpr int P1_SLOT = 32768, P1_NSLOT = 4, P1_NST = 20;
struct P1Unit { const char* base; int pitch; int kind; int col; };

template <int NU>
DI void p1_body(const char* __restrict__ xt_w, const char* b0, const char* b1, const char* b2, int p0, int p1, int p2,
                unsigned lds0, int wid, int lane, f32x16 (&acc)[2][3]) {
    constexpr int CNT = NU + 1;
    const int r8 = lane >> 3, cs = (lane & 7) ^ (((r8 >> 1) & 1) << 2);
    const unsigned vx = lane * 16;
    const unsigned v0 = r8 * p0 + cs * 16, v1 = r8 * p1 + cs * 16, v2 = r8 * p2 + cs * 16;
    const int qd = wid & 3, uo = wid >> 2;
    const unsigned dx = wid * 1024, dw = 8192 + uo * 4096 + qd * 1024;
    auto issue = [&](int st) {
        const unsigned sl = lds0 + (st & 3) * P1_SLOT;
        glds16(vx, xt_w + (unsigned)(st * 2048), sl + dx);
        glds16(v0, b0 + (unsigned)(st * 32 * p0), sl + dw);
        glds16(v1, b1 + (unsigned)(st * 32 * p1), sl + dw + 8192);
        if (NU == 3) glds16(v2, b2 + (unsigned)(st * 32 * p2), sl + dw + 16384);
    };
    const int th = wid & 1, fh = (wid >> 1) & 1, uh = wid >> 2;
    const int g = lane >> 4, q = (lane & 15) >> 2, p = lane & 3;
    const unsigned xoff = (th * 2) * 2048 + lane * 16;
    const unsigned woff = 8192 + uh * 3 * 4096 + (8 * (g >> 1) + q) * 128 + (((fh * 4 + (g & 1) * 2 + (p >> 1)) ^ (((q >> 1) & 1) << 2)) * 16) + (p & 1) * 8;
    bf16x8 xa[2], wa[NU], xb[2], wb[NU];
    auto rd = [&](int st, int ks, bf16x8 (&xf)[2], bf16x8 (&wf)[NU]) {
        lds_cptr sb = (lds_cptr)(size_t)(lds0 + (st & 3) * P1_SLOT);
#pragma unroll
        for (int t = 0; t < 2; ++t) xf[t] = *(const LAS bf16x8*)(sb + xoff + t * 2048 + ks * 1024);
#pragma unroll
        for (int u = 0; u < NU; ++u) wf[u] = cat8(vtr(sb + woff + u * 4096 + ks * 2048), vtr(sb + woff + u * 4096 + ks * 2048 + 512));
    };
    auto mm = [&](bf16x8 (&xf)[2], bf16x8 (&wf)[NU]) {
#pragma unroll
        for (int u = 0; u < NU; ++u)
#pragma unroll
            for (int t = 0; t < 2; ++t) acc[t][u] = MFMA32(wf[u], xf[t], acc[t][u]);
    };
    issue(0); issue(1); issue(2); issue(3);
    WAIT_VM(3 * CNT); BAR();
    rd(0, 0, xa, wa);
    for (int st = 0; st < P1_NST; ++st) {
        rd(st, 1, xb, wb);
        SBAR();
        mm(xa, wa);
        SBAR();
        if (st + 3 < P1_NST) { WAIT_VM(2 * CNT); } else if (st + 2 < P1_NST) { WAIT_VM(CNT); } else { WAIT_VM(0); }
        WAIT_LGKM0();
        BAR();
        if (st + 4 < P1_NST) issue(st + 4);
        if (st + 1 < P1_NST) rd(st + 1, 0, xa, wa);
        SBAR();
        mm(xb, wb);
        SBAR();
    }
}

__global__ __launch_bounds__(512, 2) void p1_k(const char* __restrict__ wqkv, const char* __restrict__ wg, const char* __restrict__ wu, char* __restrict__ ws) {
    extern __shared__ __attribute__((aligned(16))) char lds[];
    const int tid = threadIdx.x, lane = tid & 63, wid = __builtin_amdgcn_readfirstlane(tid >> 6);
    const int b = blockIdx.x, xcd = b & 7, idx = b >> 3;
    const int panel = (xcd & 1) * 8 + (idx & 7), slice = (xcd >> 1) + 4 * (idx >> 3);
    int type, qa, pa;
    if (slice < 4) { type = 0; qa = 2 * slice; pa = 2 * slice; }
    else if (slice < 11) { type = 1; qa = 8 + (slice - 4); pa = 8 + 2 * (slice - 4); }
    else { type = 2; qa = 15 + 3 * (slice - 11); pa = 22 + (slice - 11); }
    const int uo = wid >> 2, qd = wid & 3;
    const int k3 = type == 1 ? 1 : 0, k4 = type == 0 ? 1 : (type == 1 ? 2 : 0), k5 = 2;
    const int i3 = type == 1 ? pa + 1 : qa + 1, i4 = type == 2 ? qa + 2 : pa + 1, i5 = pa + 1;
    const int ka = uo == 0 ? 0 : 1, ia = uo == 0 ? qa : pa;
    const int kb = uo == 0 ? 2 : k3, ib = uo == 0 ? pa : i3;
    const int kc = uo == 0 ? k4 : k5, ic = uo == 0 ? i4 : i5;
    const int p0 = ka == 0 ? 3840 : 3456, p1 = kb == 0 ? 3840 : 3456, p2 = kc == 0 ? 3840 : 3456;
    const char* b0 = (ka == 0 ? wqkv : wg) + (unsigned)(ia * 128 + qd * 8 * p0);
    const char* b1 = (kb == 0 ? wqkv : kb == 1 ? wg : wu) + (unsigned)(ib * 128 + qd * 8 * p1);
    const char* b2 = (kc == 0 ? wqkv : kc == 1 ? wg : wu) + (unsigned)(ic * 128 + qd * 8 * p2);
    const char* xt_w = ws + WS_XT + (unsigned)(((panel * 4 + (wid >> 1)) * 40 + (wid & 1)) * 1024);
    b0 = uptr(b0); b1 = uptr(b1); b2 = uptr(b2); xt_w = uptr(xt_w);
    asm volatile("s_nop 4" ::"s"(b0), "s"(b1), "s"(b2), "s"(xt_w), "s"(p0), "s"(p1), "s"(p2));
    const unsigned lds0 = (unsigned)(size_t)lds;
    f32x16 acc[2][3];
#pragma unroll
    for (int t = 0; t < 2; ++t)
#pragma unroll
        for (int u = 0; u < 3; ++u)
#pragma unroll
            for (int r = 0; r < 16; ++r) acc[t][u][r] = 0.f;
    const bool full = (type == 0) || (uo == 0);
    if (full) p1_body<3>(xt_w, b0, b1, b2, p0, p1, p2, lds0, wid, lane, acc);
    else p1_body<2>(xt_w, b0, b1, b2, p0, p1, p2, lds0, wid, lane, acc);

    const int th = wid & 1, fh = (wid >> 1) & 1, uh = wid >> 2;
    const int c = lane & 31, h = lane >> 5;
    auto store_q = [&](const f32x16& a, int unit, int t) {
        const int tok = panel * 128 + th * 64 + t * 32 + c;
        const int f40 = unit * 16 + fh * 8 + h;
        const float sc = (unit < 10) ? 0.22360679774997896f * 1.4426950408889634f : 1.f;
#pragma unroll
        for (int g = 0; g < 4; ++g) {
            const int f4 = f40 + 2 * g;
            u32x2 w = {pk2(a[4 * g] * sc, a[4 * g + 1] * sc), pk2(a[4 * g + 2] * sc, a[4 * g + 3] * sc)};
            *(u32x2*)(ws + WS_QKV + (size_t)f4 * 16384 + tok * 8) = w;
        }
    };
    auto store_h = [&](const f32x16& gg, const f32x16& uu, int pair, int t) {
        const int tok = panel * 128 + th * 64 + t * 32 + c;
        const int ks3 = 20 + pair * 2 + fh;
        char* dst = ws + WS_ACT + ((size_t)(tok >> 4) * NKS3 + ks3) * 1024 + ((tok & 15) + 16 * h) * 16;
#pragma unroll
        for (int gp = 0; gp < 2; ++gp) {
            u32x4 w;
#pragma unroll
            for (int j = 0; j < 4; ++j) w[j] = pk2(gg[8 * gp + 2 * j] * uu[8 * gp + 2 * j], gg[8 * gp + 2 * j + 1] * uu[8 * gp + 2 * j + 1]);
            *(u32x4*)(dst + gp * 512) = w;
        }
    };
#pragma unroll
    for (int t = 0; t < 2; ++t) {
        if (uh == 0 || type == 0) { store_q(acc[t][0], qa + uh, t); store_h(acc[t][1], acc[t][2], pa + uh, t); }
        else if (type == 1) { store_h(acc[t][0], acc[t][1], pa + 1, t); }
        else { store_q(acc[t][0], qa + 1, t); store_q(acc[t][1], qa + 2, t); }
    }
}

constexpr int AT_PL = 4096 + 32;
constexpr int AT_V = 5 * AT_PL, AT_ONES = 10 * AT_PL, AT_LDS = AT_ONES + 64;

__global__ __launch_bounds__(512, 2) void attn_k(char* __restrict__ ws) {
    extern __shared__ __attribute__((aligned(16))) char lds[];
    const int tid = threadIdx.x, lane = tid & 63, wid = __builtin_amdgcn_readfirstlane(tid >> 6);
    const int b = blockIdx.x, xcd = b & 7, idx = b >> 3;
    const int qb = xcd, head = idx;
    const unsigned lds0 = (unsigned)(size_t)lds;
    const char* qkv = ws + WS_QKV;
    {
        const int kb0 = 256 * qb - 256;
#pragma unroll
        for (int i = 0; i < 5; ++i) {
            const int pc = wid + 8 * i, pl = pc >> 2, qu = pc & 3;
            int key0 = kb0 + qu * 128; if (key0 < 0) key0 += 256;
            const int gp = (pl < 5 ? 160 : 320 - 5) + head * 5 + pl;
            glds16(lane * 16, qkv + (unsigned)(gp * 16384 + key0 * 8), lds0 + pl * AT_PL + qu * 1024);
        }
    }
    if (tid < 2) *(LAS u32x2*)(size_t)(lds0 + AT_ONES + tid * 8) = (u32x2){0x00003f80u, 0u};
    const int c = lane & 31, h = lane >> 5;
    const int q0 = 256 * qb + 32 * wid;
    bf16x8 qf0, qf1;
    {
        const char* qp = qkv + (size_t)(head * 5) * 16384 + (q0 + c) * 8;
        const u32x2 a = *(const u32x2*)(qp + (2 * h) * 16384), bq = *(const u32x2*)(qp + (2 * h + 1) * 16384), e = *(const u32x2*)(qp + 4 * 16384);
        qf0 = __builtin_bit_cast(bf16x8, (u32x4){a.x, a.y, bq.x, bq.y});
        qf1 = __builtin_bit_cast(bf16x8, (u32x4){h ? 0u : e.x, h ? 0u : e.y, 0u, 0u});
    }
    WAIT_VM(0); WAIT_LGKM0(); BAR();
    f32x16 s[9];
    lds_cptr kp = (lds_cptr)(size_t)(lds0 + (2 * h) * AT_PL + (32 * wid + c) * 8);
    lds_cptr kp4 = (lds_cptr)(size_t)(lds0 + 4 * AT_PL + (32 * wid + c) * 8);
    f32x16 zero;
#pragma unroll
    for (int r = 0; r < 16; ++r) zero[r] = 0.f;
#pragma unroll
    for (int j = 0; j < 9; ++j) {
        const u32x2 a = *(const LAS u32x2*)(kp + j * 256), bq = *(const LAS u32x2*)(kp + AT_PL + j * 256), e = *(const LAS u32x2*)(kp4 + j * 256);
        const bf16x8 k0 = __builtin_bit_cast(bf16x8, (u32x4){a.x, a.y, bq.x, bq.y});
        const bf16x8 k1 = __builtin_bit_cast(bf16x8, (u32x4){e.x, e.y, e.x, e.y});
        s[j] = MFMA32(k0, qf0, zero);
        s[j] = MFMA32(k1, qf1, s[j]);
    }
    const float NEG = -__builtin_inff();
    const int ch = c - 4 * h;
#pragma unroll
    for (int r = 0; r < 16; ++r) {
        const int rr = (r & 3) + 8 * (r >> 2);
        if (!(rr > ch)) s[0][r] = NEG;
        if (rr > ch) s[8][r] = NEG;
    }
    if (qb == 0) {
#pragma unroll
        for (int j = 0; j < 8; ++j)
            if (j < 8 - wid) {
#pragma unroll
                for (int r = 0; r < 16; ++r) s[j][r] = NEG;
            }
    }
    float m = NEG;
#pragma unroll
    for (int j = 0; j < 9; ++j)
#pragma unroll
        for (int r = 0; r < 16; r += 2) m = __builtin_fmaxf(__builtin_fmaxf(m, s[j][r]), s[j][r + 1]);
    { auto rr = __builtin_amdgcn_permlane32_swap(__float_as_uint(m), __float_as_uint(m), false, false);
      m = __builtin_fmaxf(__uint_as_float(rr[0]), __uint_as_float(rr[1])); }
    const int g = lane >> 4, q = (lane & 15) >> 2, p = lane & 3;
    const int dq = 4 * (g & 1) + p;
    const bool real = dq < 5;
    const unsigned vbase = real ? lds0 + AT_V + dq * AT_PL + (32 * wid + 4 * h + q) * 8 : lds0 + AT_ONES + (dq == 5 ? 0 : 8);
    const unsigned vstep = real ? 64u : 0u;
    f32x16 o = zero;
#pragma unroll
    for (int j = 0; j < 9; ++j) {
#pragma unroll
        for (int r = 0; r < 16; ++r) s[j][r] = __builtin_amdgcn_exp2f(s[j][r] - m);
#pragma unroll
        for (int ss = 0; ss < 2; ++ss) {
            u32x4 pw;
#pragma unroll
            for (int i = 0; i < 4; ++i) pw[i] = pk2(s[j][8 * ss + 2 * i], s[j][8 * ss + 2 * i + 1]);
            const s16x4 lo = vtr((lds_cptr)(size_t)(vbase + vstep * (4 * j + 2 * ss)));
            const s16x4 hi = vtr((lds_cptr)(size_t)(vbase + vstep * (4 * j + 2 * ss + 1)));
            o = MFMA32(cat8(lo, hi), __builtin_bit_cast(bf16x8, pw), o);
        }
    }
    float l; { auto rr = __builtin_amdgcn_permlane32_swap(__float_as_uint(o[8]), __float_as_uint(o[8]), false, false); l = h ? o[8] : __uint_as_float(rr[1]); }
    const float inv = 1.f / l;
    const int tok = q0 + c;
    const int a5 = (head >> 1) * 5, odd = head & 1;
    char* act = ws + WS_ACT + (size_t)(tok >> 4) * NKS3 * 1024 + (tok & 15) * 16;
    {
        const int slot = a5 + odd * 2 + h;
        u32x4 w = {pk2(o[0] * inv, o[1] * inv), pk2(o[2] * inv, o[3] * inv), pk2(o[4] * inv, o[5] * inv), pk2(o[6] * inv, o[7] * inv)};
        *(u32x4*)(act + (slot >> 2) * 1024 + (slot & 3) * 256) = w;
        if (h == 0) {
            const int s4 = a5 + 4;
            u32x2 w2 = {pk2(o[8] * inv, o[9] * inv), pk2(o[10] * inv, o[11] * inv)};
            *(u32x2*)(act + (s4 >> 2) * 1024 + (s4 & 3) * 256 + odd * 8) = w2;
        }
    }
}

constexpr int P3_SLOT = 36864, P3_NSLOT = 4, P3_NST = 19;

__global__ __launch_bounds__(256, 1) void p3_k(const u16* __restrict__ x, const char* __restrict__ ws, u16* __restrict__ out) {
    extern __shared__ __attribute__((aligned(16))) char lds[];
    const int tid = threadIdx.x, lane = tid & 63, wid = __builtin_amdgcn_readfirstlane(tid >> 6);
    const int b = blockIdx.x, xcd = b & 7, idx = b >> 3;
    const int mt = xcd * 4 + (idx & 3), nt = idx >> 2;
    const unsigned lds0 = (unsigned)(size_t)lds;
    const char* act = ws + WS_ACT + (unsigned)((mt * 4 + wid) * NKS3 * 1024);
    const char* wt = ws + WS_WT3 + (unsigned)(nt * NKS3 * 5 * 1024 + wid * 5 * 1024);
    const unsigned vl = lane * 16;
    auto issue = [&](int st, int nks) {
        const unsigned sl = lds0 + (st & 3) * P3_SLOT;
        if (wid < nks) {
#pragma unroll
            for (int i = 0; i < 5; ++i) glds16(vl, wt + (unsigned)(st * 20480 + i * 1024), sl + wid * 5120 + i * 1024);
        }
#pragma unroll
        for (int i = 0; i < 4; ++i) if (i < nks) glds16(vl, act + (unsigned)((st * 4 + i) * 1024), sl + 20480 + wid * 4096 + i * 1024);
    };
    f32x4 acc[5];
#pragma unroll
    for (int i = 0; i < 5; ++i) acc[i] = (f32x4){0.f, 0.f, 0.f, 0.f};
    issue(0, 4); issue(1, 4); issue(2, 4);
    for (int st = 0; st < P3_NST; ++st) {
        if (st + 3 < P3_NST) { WAIT_VM(18); } else { WAIT_VM(0); }
        WAIT_LGKM0();
        BAR();
        if (st + 3 < P3_NST) issue(st + 3, (st + 3 == P3_NST - 1) ? 2 : 4);
        lds_cptr sb = (lds_cptr)(size_t)(lds0 + (st & 3) * P3_SLOT) + vl;
        const int nks = (st == P3_NST - 1) ? 2 : 4;
#pragma unroll
        for (int ks = 0; ks < 4; ++ks) {
            if (ks < nks) {
                const bf16x8 af = *(const LAS bf16x8*)(sb + 20480 + wid * 4096 + ks * 1024);
#pragma unroll
                for (int ng = 0; ng < 5; ++ng) {
                    const bf16x8 wf = *(const LAS bf16x8*)(sb + ks * 5120 + ng * 1024);
                    acc[ng] = MFMA16(wf, af, acc[ng]);
                }
            }
        }
    }
    const int tok = mt * 64 + wid * 16 + (lane & 15), rq = lane >> 4;
    const size_t off = (size_t)tok * D_ + nt * 80 + rq * 20;
#pragma unroll
    for (int ng = 0; ng < 5; ++ng) {
        const u32x2 xr = *(const u32x2*)(x + off + ng * 4);
        const float r0 = __uint_as_float(xr.x << 16) + acc[ng][0], r1 = __uint_as_float(xr.x & 0xffff0000u) + acc[ng][1];
        const float r2 = __uint_as_float(xr.y << 16) + acc[ng][2], r3 = __uint_as_float(xr.y & 0xffff0000u) + acc[ng][3];
        *(u32x2*)(out + off + ng * 4) = (u32x2){pk2(r0, r1), pk2(r2, r3)};
    }
}

extern "C" void kernel_launch(void* const* d_in, const int* in_sizes, int n_in, void* d_out, int out_size, void* d_ws, size_t ws_size, hipStream_t stream) {
    const u16* x = (const u16*)d_in[0];
    const char* wqkv = (const char*)d_in[1];
    const u16* wo = (const u16*)d_in[2];
    const char* wg = (const char*)d_in[4];
    const char* wu = (const char*)d_in[5];
    const u16* wd = (const u16*)d_in[6];
    char* ws = (char*)d_ws;
    (void)hipFuncSetAttribute((const void*)p1_k, hipFuncAttributeMaxDynamicSharedMemorySize, P1_SLOT * P1_NSLOT);
    (void)hipFuncSetAttribute((const void*)p3_k, hipFuncAttributeMaxDynamicSharedMemorySize, P3_SLOT * P3_NSLOT);
    prep_k<<<256 + 148, 256, 0, stream>>>(x, wo, wd, ws);
    p1_k<<<256, 512, P1_SLOT * P1_NSLOT, stream>>>(wqkv, wg, wu, ws);
    attn_k<<<256, 512, AT_LDS, stream>>>(ws);
    p3_k<<<256, 256, P3_SLOT * P3_NSLOT, stream>>>(x, ws, (u16*)d_out);
}
```

```cpp
#include <hip/hip_runtime.h>
#include <hip/hip_bf16.h>
#include <stdint.h>

typedef unsigned short u16;
typedef short bf16x8 __attribute__((ext_vector_type(8)));
typedef short s16x4 __attribute__((ext_vector_type(4)));
typedef float f32x16 __attribute__((ext_vector_type(16)));
typedef float f32x4 __attribute__((ext_vector_type(4)));
typedef float f32x2 __attribute__((ext_vector_type(2)));
typedef unsigned u32x4 __attribute__((ext_vector_type(4)));
typedef unsigned u32x2 __attribute__((ext_vector_type(2)));
typedef __bf16 bf16x2_t __attribute__((ext_vector_type(2)));
#define LAS __attribute__((address_space(3)))
#define DI __device__ __forceinline__
typedef LAS const char* lds_cptr;
typedef short v4i16_t __attribute__((ext_vector_type(4)));

constexpr int S_ = 2048, D_ = 640, NH = 32, DH = 20, F_ = 1728;
constexpr int NKS3 = 74;
constexpr size_t WS_XT = 0;
constexpr size_t WS_QKV = WS_XT + (size_t)64 * 40 * 1024;
constexpr size_t WS_ACT = WS_QKV + (size_t)480 * 16384;
constexpr size_t WS_WT3 = WS_ACT + (size_t)128 * NKS3 * 1024;
constexpr size_t WS_END = WS_WT3 + (size_t)8 * NKS3 * 5 * 1024;

DI float bf2f(u16 b) { return __uint_as_float(((unsigned)b) << 16); }
DI unsigned pk2(float a, float b) { f32x2 v = {a, b}; bf16x2_t r = __builtin_convertvector(v, bf16x2_t); return __builtin_bit_cast(unsigned, r); }
DI s16x4 vtr(lds_cptr p) { return __builtin_bit_cast(s16x4, __builtin_amdgcn_ds_read_tr16_b64_v4i16((LAS v4i16_t*)p)); }
DI bf16x8 cat8(s16x4 a, s16x4 b) { return __builtin_shufflevector(a, b, 0, 1, 2, 3, 4, 5, 6, 7); }
DI void glds16(unsigned voff, const void* sbase, unsigned dst) {
    unsigned sv;
    asm volatile("s_mov_b32 %0, m0\n\ts_mov_b32 m0, %3\n\ts_nop 0\n\tglobal_load_lds_dwordx4 %1, %2\n\ts_mov_b32 m0, %0"
                 : "=&s"(sv) : "v"(voff), "s"(sbase), "s"(dst) : "memory");
}
typedef int srd_t __attribute__((ext_vector_type(4)));
DI srd_t make_srd(const void* base, unsigned bytes) {
    const uint64_t v = (uint64_t)base;
    srd_t r; r[0] = (int)(unsigned)v; r[1] = (int)((unsigned)(v >> 32) & 0xffffu); r[2] = (int)bytes; r[3] = 0x00020000; return r;
}
template <int OFF> DI void bdma(unsigned voff, srd_t srd, unsigned soff, unsigned dst) {
    asm volatile("s_mov_b32 m0, %3\n\ts_nop 0\n\tbuffer_load_dwordx4 %0, %1, %2 offen offset:%4 lds" ::"v"(voff), "s"(srd), "s"(soff), "s"(dst), "n"(OFF) : "memory");
}
template <int OFF> DI void bdma_m0(unsigned voff, srd_t srd, unsigned soff) {
    asm volatile("buffer_load_dwordx4 %0, %1, %2 offen offset:%3 lds" ::"v"(voff), "s"(srd), "s"(soff), "n"(OFF) : "memory");
}
DI const char* uptr(const char* p) {
    const uint64_t v = (uint64_t)p;
    const unsigned lo = __builtin_amdgcn_readfirstlane((unsigned)v), hi = __builtin_amdgcn_readfirstlane((unsigned)(v >> 32));
    return (const char*)(((uint64_t)hi << 32) | lo);
}
#define ST(i)
#define ST_DECL
#define ST_OUT(kid)
#define WAIT_VM(n) asm volatile("s_waitcnt vmcnt(%0)" ::"n"(n) : "memory")
#define WAIT_LGKM0() asm volatile("s_waitcnt lgkmcnt(0)" ::: "memory")
#define BAR() do { asm volatile("" ::: "memory"); __builtin_amdgcn_s_barrier(); asm volatile("" ::: "memory"); } while (0)
#define SBAR() __builtin_amdgcn_sched_barrier(0)
#define MFMA32(a, b, c) __builtin_amdgcn_mfma_f32_32x32x16_bf16((a), (b), (c), 0, 0, 0)
#define MFMA16(a, b, c) __builtin_amdgcn_mfma_f32_16x16x32_bf16((a), (b), (c), 0, 0, 0)

__global__ __launch_bounds__(256) void prep_k(const u16* __restrict__ x, const u16* __restrict__ wo, const u16* __restrict__ wd, char* __restrict__ ws) {
    __shared__ __attribute__((aligned(16))) u16 sm[32 * 328];
    const int tid = threadIdx.x, lane = tid & 63, wid = tid >> 6;
    if (blockIdx.x < 256) {
        const int t0 = blockIdx.x * 8;
#pragma unroll
        for (int r = 0; r < 2; ++r) {
            const int tok = t0 + wid * 2 + r;
            const unsigned* row = (const unsigned*)(x + (size_t)tok * D_);
            float v[10]; float s = 0.f;
#pragma unroll
            for (int i = 0; i < 5; ++i) { unsigned w = row[lane + 64 * i]; v[2 * i] = __uint_as_float(w << 16); v[2 * i + 1] = __uint_as_float(w & 0xffff0000u); s += v[2 * i] + v[2 * i + 1]; }
#pragma unroll
            for (int o = 32; o >= 1; o >>= 1) s += __shfl_xor(s, o);
            const float m = s * (1.f / D_); float q = 0.f;
#pragma unroll
            for (int i = 0; i < 10; ++i) { const float d = v[i] - m; q += d * d; }
#pragma unroll
            for (int o = 32; o >= 1; o >>= 1) q += __shfl_xor(q, o);
            const float rs = rsqrtf(q * (1.f / D_) + 1e-5f);
#pragma unroll
            for (int i = 0; i < 5; ++i) ((unsigned*)sm)[(wid * 2 + r) * 324 + lane + 64 * i] = pk2((v[2 * i] - m) * rs, (v[2 * i + 1] - m) * rs);
        }
        __syncthreads();
        char* xt = ws + WS_XT + (size_t)(t0 >> 5) * 40 * 1024;
        for (int i = tid; i < 640; i += 256) {
            const int tk = i & 7, c = i >> 3;
            const u32x4 v = *(const u32x4*)((const char*)sm + tk * 1296 + c * 16);
            *(u32x4*)(xt + (size_t)(c >> 1) * 1024 + ((c & 1) * 32 + (t0 & 31) + tk) * 16) = v;
        }
    } else {
        const int u = blockIdx.x - 256;
        const int ks = u >> 1, ch = u & 1;
        for (int i = tid; i < 32 * 40; i += 256) {
            const int sr = i / 40, c8 = i % 40;
            const int kg = sr >> 3, j = sr & 7;
            const u16* src;
            if (ks < 20) {
                const int slot = 4 * ks + kg, a = slot / 5, ii = slot % 5;
                int head, d;
                if (ii < 4) { head = 2 * a + (ii >> 1); d = (ii & 1) * 4 + (j & 3) + 8 * (j >> 2); }
                else { head = 2 * a + (j >> 2); d = 16 + (j & 3); }
                src = wo + (size_t)(head * DH + d) * D_;
            } else {
                const int k = 32 * (ks - 20) + 16 * (kg >> 1) + 8 * (j >> 2) + 4 * (kg & 1) + (j & 3);
                src = wd + (size_t)k * D_;
            }
            *(u32x4*)(sm + sr * 328 + c8 * 8) = *(const u32x4*)(src + ch * 320 + c8 * 8);
        }
        __syncthreads();
        for (int i = tid; i < 4 * 5 * 64; i += 256) {
            const int l = i & 63, ng = (i >> 6) % 5, ntl = (i >> 6) / 5;
            const int n = l & 15, kg = l >> 4;
            const int col = ntl * 80 + 20 * (n >> 2) + 4 * ng + (n & 3);
            unsigned w[4];
#pragma unroll
            for (int j = 0; j < 4; ++j) w[j] = (unsigned)sm[(kg * 8 + 2 * j) * 328 + col] | ((unsigned)sm[(kg * 8 + 2 * j + 1) * 328 + col] << 16);
            const int nt = ch * 4 + ntl;
            *(u32x4*)(ws + WS_WT3 + ((size_t)(nt * NKS3 + ks) * 5 + ng) * 1024 + l * 16) = (u32x4){w[0], w[1], w[2], w[3]};
        }
    }
}

constexpr int P1_SLOT = 32768, P1_NSLOT = 4, P1_NST = 20;

template <int NU>
DI void p1_compute(unsigned lds0, int wid, int lane, f32x16 (&acc)[2][3]) {
    const int th = wid & 1, fh = (wid >> 1) & 1, uh = wid >> 2;
    const int g = lane >> 4, q = (lane & 15) >> 2, p = lane & 3;
    const unsigned xoff = lds0 + (th * 2) * 2048 + lane * 16;
    const unsigned woff = lds0 + 8192 + uh * 3 * 4096 + (8 * (g >> 1) + q) * 128 + (((fh * 4 + (g & 1) * 2 + (p >> 1)) ^ (((q >> 1) & 1) << 2)) * 16) + (p & 1) * 8;
    bf16x8 xa[2], wa[NU], xb[2], wb[NU];
#define RDX(dst, t, ks) dst = *(const LAS bf16x8*)(size_t)(xs + (t) * 2048 + (ks) * 1024)
#define RDW(dst, u, ks) dst = cat8(vtr((lds_cptr)(size_t)(wsb + (u) * 4096 + (ks) * 2048)), vtr((lds_cptr)(size_t)(wsb + (u) * 4096 + (ks) * 2048 + 512)))
#define PHASE(xf, wf, nx, nw, ks) do { \
        acc[0][0] = MFMA32(wf[0], xf[0], acc[0][0]); SBAR(); RDX(nx[0], 0, ks); RDX(nx[1], 1, ks); SBAR(); \
        acc[1][0] = MFMA32(wf[0], xf[1], acc[1][0]); SBAR(); RDW(nw[0], 0, ks); SBAR(); \
        acc[0][1] = MFMA32(wf[1], xf[0], acc[0][1]); SBAR(); RDW(nw[1], 1, ks); SBAR(); \
        acc[1][1] = MFMA32(wf[1], xf[1], acc[1][1]); SBAR(); \
        if (NU == 3) { RDW(nw[NU - 1], 2, ks); SBAR(); acc[0][2] = MFMA32(wf[NU - 1], xf[0], acc[0][2]); acc[1][2] = MFMA32(wf[NU - 1], xf[1], acc[1][2]); SBAR(); } } while (0)
    BAR();
    { const unsigned xs = xoff, wsb = woff; RDX(xa[0], 0, 0); RDX(xa[1], 1, 0);
#pragma unroll
      for (int u = 0; u < NU; ++u) RDW(wa[u], u, 0); }
    for (int st = 0; st < P1_NST; ++st) {
        { const unsigned so = (st & 3) * P1_SLOT; const unsigned xs = xoff + so, wsb = woff + so;
          PHASE(xa, wa, xb, wb, 1); }
        WAIT_LGKM0();
        BAR();
        { const unsigned so = ((st + 1) & 3) * P1_SLOT; const unsigned xs = xoff + so, wsb = woff + so;
          PHASE(xb, wb, xa, wa, 0); }
    }
#undef PHASE
#undef RDX
#undef RDW
}

__global__ __launch_bounds__(768, 3) void p1_k(const char* __restrict__ wqkv, const char* __restrict__ wg, const char* __restrict__ wu, char* __restrict__ ws) {
    extern __shared__ __attribute__((aligned(16))) char lds[];
    ST_DECL ST(0); char* ws_dbg = ws;
    const int tid = threadIdx.x, lane = tid & 63, wid = __builtin_amdgcn_readfirstlane(tid >> 6);
    const int b = blockIdx.x, xcd = b & 7, idx = b >> 3;
    const int panel = (xcd & 1) * 8 + (idx & 7), slice = (xcd >> 1) + 4 * (idx >> 3);
    int type, qa, pa;
    if (slice < 4) { type = 0; qa = 2 * slice; pa = 2 * slice; }
    else if (slice < 11) { type = 1; qa = 8 + (slice - 4); pa = 8 + 2 * (slice - 4); }
    else { type = 2; qa = 15 + 3 * (slice - 11); pa = 22 + (slice - 11); }
    const unsigned lds0 = (unsigned)(size_t)lds;
    if (wid >= 8) {
        const int l = wid - 8;
        const int k3 = type == 1 ? 1 : 0, k4 = type == 0 ? 1 : (type == 1 ? 2 : 0);
        const int i3 = type == 1 ? pa + 1 : qa + 1, i4 = type == 2 ? qa + 2 : pa + 1;
        constexpr unsigned QB = 640u * 3840u, GB = 640u * 3456u;
        const srd_t s0 = make_srd(wqkv + qa * 128, QB - qa * 128), s1 = make_srd(wg + pa * 128, GB - pa * 128), s2 = make_srd(wu + pa * 128, GB - pa * 128);
        const srd_t s3 = make_srd((k3 == 0 ? wqkv : wg) + i3 * 128, (k3 == 0 ? QB : GB) - i3 * 128);
        const srd_t s4 = make_srd((k4 == 0 ? wqkv : k4 == 1 ? wg : wu) + i4 * 128, (k4 == 0 ? QB : GB) - i4 * 128);
        const srd_t s5 = make_srd(wu + (type == 0 ? (pa + 1) * 128 : 0), type == 0 ? GB - (pa + 1) * 128 : 0u);
        const srd_t sx = make_srd(ws + WS_XT + (unsigned)((panel * 4 + l) * 40 * 1024), 40 * 1024);
        const int r8 = lane >> 3, cs = (lane & 7) ^ (((r8 >> 1) & 1) << 2);
        const unsigned vq = r8 * 3840 + cs * 16, vg = r8 * 3456 + cs * 16, vx = lane * 16;
        const unsigned v3 = k3 == 0 ? vq : vg, v4 = k4 == 0 ? vq : vg;
        const unsigned p3 = k3 == 0 ? 3840 : 3456, p4 = k4 == 0 ? 3840 : 3456;
        auto issue = [&](int st) {
            const unsigned sl = lds0 + (st & 3) * P1_SLOT;
            const unsigned row = st * 32 + l * 8;
            bdma<0>(vx, sx, st * 2048, sl + l * 2048); bdma_m0<1024>(vx, sx, st * 2048);
            const unsigned dw = sl + 8192 + l * 1024;
            bdma<0>(vq, s0, row * 3840, dw);
            bdma<0>(vg, s1, row * 3456, dw + 4096);
            bdma<0>(vg, s2, row * 3456, dw + 8192);
            bdma<0>(v3, s3, row * p3, dw + 12288);
            bdma<0>(v4, s4, row * p4, dw + 16384);
            bdma<0>(vg, s5, row * 3456, dw + 20480);
        };
        issue(0); issue(1); issue(2);
        for (int st = 0; st <= P1_NST; ++st) {
            WAIT_VM(16);
            BAR();
            issue(st + 3);
        }
        WAIT_VM(0);
        return;
    }
    f32x16 acc[2][3];
#pragma unroll
    for (int t = 0; t < 2; ++t)
#pragma unroll
        for (int u = 0; u < 3; ++u)
#pragma unroll
            for (int r = 0; r < 16; ++r) acc[t][u][r] = 0.f;
    const int th = wid & 1, fh = (wid >> 1) & 1, uh = wid >> 2;
    ST(1);
    if (type == 0 || uh == 0) p1_compute<3>(lds0, wid, lane, acc);
    else p1_compute<2>(lds0, wid, lane, acc);
    ST(3);

    const int c = lane & 31, h = lane >> 5;
    auto store_q = [&](const f32x16& a, int unit, int t) {
        const int tok = panel * 128 + th * 64 + t * 32 + c;
        const int f40 = unit * 16 + fh * 8 + h;
        const float sc = (unit < 10) ? 0.22360679774997896f * 1.4426950408889634f : 1.f;
#pragma unroll
        for (int g = 0; g < 4; ++g) {
            const int f4 = f40 + 2 * g;
            u32x2 w = {pk2(a[4 * g] * sc, a[4 * g + 1] * sc), pk2(a[4 * g + 2] * sc, a[4 * g + 3] * sc)};
            *(u32x2*)(ws + WS_QKV + (size_t)f4 * 16384 + tok * 8) = w;
        }
    };
    auto store_h = [&](const f32x16& gg, const f32x16& uu, int pair, int t) {
        const int tok = panel * 128 + th * 64 + t * 32 + c;
        const int ks3 = 20 + pair * 2 + fh;
        char* dst = ws + WS_ACT + ((size_t)(tok >> 4) * NKS3 + ks3) * 1024 + ((tok & 15) + 16 * h) * 16;
#pragma unroll
        for (int gp = 0; gp < 2; ++gp) {
            u32x4 w;
#pragma unroll
            for (int j = 0; j < 4; ++j) w[j] = pk2(gg[8 * gp + 2 * j] * uu[8 * gp + 2 * j], gg[8 * gp + 2 * j + 1] * uu[8 * gp + 2 * j + 1]);
            *(u32x4*)(dst + gp * 512) = w;
        }
    };
#pragma unroll
    for (int t = 0; t < 2; ++t) {
        if (uh == 0 || type == 0) { store_q(acc[t][0], qa + uh, t); store_h(acc[t][1], acc[t][2], pa + uh, t); }
        else if (type == 1) { store_h(acc[t][0], acc[t][1], pa + 1, t); }
        else { store_q(acc[t][0], qa + 1, t); store_q(acc[t][1], qa + 2, t); }
    }
    ST(4);
    WAIT_VM(0);
    ST(5);
    ST_OUT(1);
}

constexpr int AT_PL = 4096 + 32;
constexpr int AT_V = 5 * AT_PL, AT_ONES = 10 * AT_PL, AT_LDS = AT_ONES + 64;

__global__ __launch_bounds__(512, 2) void attn_k(char* __restrict__ ws) {
    extern __shared__ __attribute__((aligned(16))) char lds[];
    ST_DECL ST(0); char* ws_dbg = ws;
    const int tid = threadIdx.x, lane = tid & 63, wid = __builtin_amdgcn_readfirstlane(tid >> 6);
    const int b = blockIdx.x, xcd = b & 7, idx = b >> 3;
    const int qb = xcd, head = idx;
    const unsigned lds0 = (unsigned)(size_t)lds;
    const char* qkv = ws + WS_QKV;
    {
        const int kb0 = 256 * qb - 256;
#pragma unroll
        for (int i = 0; i < 5; ++i) {
            const int pc = wid + 8 * i, pl = pc >> 2, qu = pc & 3;
            int key0 = kb0 + qu * 128; if (key0 < 0) key0 += 256;
            const int gp = (pl < 5 ? 160 : 320 - 5) + head * 5 + pl;
            glds16(lane * 16, qkv + (unsigned)(gp * 16384 + key0 * 8), lds0 + pl * AT_PL + qu * 1024);
        }
    }
    if (tid < 2) *(LAS u32x2*)(size_t)(lds0 + AT_ONES + tid * 8) = (u32x2){0x00003f80u, 0u};
    const int c = lane & 31, h = lane >> 5;
    const int q0 = 256 * qb + 32 * wid;
    bf16x8 qf0, qf1;
    {
        const char* qp = qkv + (size_t)(head * 5) * 16384 + (q0 + c) * 8;
        const u32x2 a = *(const u32x2*)(qp + (2 * h) * 16384), bq = *(const u32x2*)(qp + (2 * h + 1) * 16384), e = *(const u32x2*)(qp + 4 * 16384);
        qf0 = __builtin_bit_cast(bf16x8, (u32x4){a.x, a.y, bq.x, bq.y});
        qf1 = __builtin_bit_cast(bf16x8, (u32x4){h ? 0u : e.x, h ? 0u : e.y, 0u, 0u});
    }
    ST(1);
    WAIT_VM(0); WAIT_LGKM0(); BAR();
    ST(2);
    f32x16 s[9];
    lds_cptr kp = (lds_cptr)(size_t)(lds0 + (2 * h) * AT_PL + (32 * wid + c) * 8);
    lds_cptr kp4 = (lds_cptr)(size_t)(lds0 + 4 * AT_PL + (32 * wid + c) * 8);
    f32x16 zero;
#pragma unroll
    for (int r = 0; r < 16; ++r) zero[r] = 0.f;
#pragma unroll
    for (int j = 0; j < 9; ++j) {
        const u32x2 a = *(const LAS u32x2*)(kp + j * 256), bq = *(const LAS u32x2*)(kp + AT_PL + j * 256), e = *(const LAS u32x2*)(kp4 + j * 256);
        const bf16x8 k0 = __builtin_bit_cast(bf16x8, (u32x4){a.x, a.y, bq.x, bq.y});
        const bf16x8 k1 = __builtin_bit_cast(bf16x8, (u32x4){e.x, e.y, e.x, e.y});
        s[j] = MFMA32(k0, qf0, zero);
        s[j] = MFMA32(k1, qf1, s[j]);
    }
    const float NEG = -__builtin_inff();
    const int ch = c - 4 * h;
#pragma unroll
    for (int r = 0; r < 16; ++r) {
        const int rr = (r & 3) + 8 * (r >> 2);
        if (!(rr > ch)) s[0][r] = NEG;
        if (rr > ch) s[8][r] = NEG;
    }
    if (qb == 0) {
#pragma unroll
        for (int j = 0; j < 8; ++j)
            if (j < 8 - wid) {
#pragma unroll
                for (int r = 0; r < 16; ++r) s[j][r] = NEG;
            }
    }
    float m = NEG;
#pragma unroll
    for (int j = 0; j < 9; ++j)
#pragma unroll
        for (int r = 0; r < 16; r += 2) m = __builtin_fmaxf(__builtin_fmaxf(m, s[j][r]), s[j][r + 1]);
    { auto rr = __builtin_amdgcn_permlane32_swap(__float_as_uint(m), __float_as_uint(m), false, false);
      m = __builtin_fmaxf(__uint_as_float(rr[0]), __uint_as_float(rr[1])); }
    ST(3);
    const int g = lane >> 4, q = (lane & 15) >> 2, p = lane & 3;
    const int dq = 4 * (g & 1) + p;
    const bool real = dq < 5;
    const unsigned vbase = real ? lds0 + AT_V + dq * AT_PL + (32 * wid + 4 * h + q) * 8 : lds0 + AT_ONES + (dq == 5 ? 0 : 8);
    const unsigned vstep = real ? 64u : 0u;
    f32x16 o = zero;
#pragma unroll
    for (int j = 0; j < 9; ++j) {
#pragma unroll
        for (int r = 0; r < 16; ++r) s[j][r] = __builtin_amdgcn_exp2f(s[j][r] - m);
#pragma unroll
        for (int ss = 0; ss < 2; ++ss) {
            u32x4 pw;
#pragma unroll
            for (int i = 0; i < 4; ++i) pw[i] = pk2(s[j][8 * ss + 2 * i], s[j][8 * ss + 2 * i + 1]);
            const s16x4 lo = vtr((lds_cptr)(size_t)(vbase + vstep * (4 * j + 2 * ss)));
            const s16x4 hi = vtr((lds_cptr)(size_t)(vbase + vstep * (4 * j + 2 * ss + 1)));
            o = MFMA32(cat8(lo, hi), __builtin_bit_cast(bf16x8, pw), o);
        }
    }
    ST(4);
    float l; { auto rr = __builtin_amdgcn_permlane32_swap(__float_as_uint(o[8]), __float_as_uint(o[8]), false, false); l = h ? o[8] : __uint_as_float(rr[1]); }
    const float inv = 1.f / l;
    const int tok = q0 + c;
    const int a5 = (head >> 1) * 5, odd = head & 1;
    char* act = ws + WS_ACT + (size_t)(tok >> 4) * NKS3 * 1024 + (tok & 15) * 16;
    {
        const int slot = a5 + odd * 2 + h;
        u32x4 w = {pk2(o[0] * inv, o[1] * inv), pk2(o[2] * inv, o[3] * inv), pk2(o[4] * inv, o[5] * inv), pk2(o[6] * inv, o[7] * inv)};
        *(u32x4*)(act + (slot >> 2) * 1024 + (slot & 3) * 256) = w;
        if (h == 0) {
            const int s4 = a5 + 4;
            u32x2 w2 = {pk2(o[8] * inv, o[9] * inv), pk2(o[10] * inv, o[11] * inv)};
            *(u32x2*)(act + (s4 >> 2) * 1024 + (s4 & 3) * 256 + odd * 8) = w2;
        }
    }
    ST(5);
    WAIT_VM(0);
    ST(6);
    ST_OUT(2);
}

constexpr int P3_SLOT = 36864, P3_NSLOT = 4, P3_NST = 19;

__global__ __launch_bounds__(512, 2) void p3_k(const u16* __restrict__ x, const char* __restrict__ ws, u16* __restrict__ out) {
    extern __shared__ __attribute__((aligned(16))) char lds[];
    ST_DECL ST(0); char* ws_dbg = (char*)ws;
    const int tid = threadIdx.x, lane = tid & 63, wid = __builtin_amdgcn_readfirstlane(tid >> 6);
    const int b = blockIdx.x, xcd = b & 7, idx = b >> 3;
    const int mt = xcd * 4 + (idx & 3), nt = idx >> 2;
    const unsigned lds0 = (unsigned)(size_t)lds;
    const unsigned vl = lane * 16;
    if (wid >= 4) {
        const int l = wid - 4;
        const srd_t sa = make_srd(ws + WS_ACT + (unsigned)((mt * 4 + l) * NKS3 * 1024), NKS3 * 1024);
        const srd_t sw = make_srd(ws + WS_WT3 + (unsigned)(nt * NKS3 * 5 * 1024), NKS3 * 5 * 1024);
        auto issue = [&](int st) {
            const unsigned sl = lds0 + (st & 3) * P3_SLOT;
            const unsigned ow = st * 20480 + l * 5120;
            bdma<0>(vl, sw, ow, sl + l * 5120); bdma_m0<1024>(vl, sw, ow); bdma_m0<2048>(vl, sw, ow); bdma_m0<3072>(vl, sw, ow);
            bdma<0>(vl, sw, ow + 4096, sl + l * 5120 + 4096);
            bdma<0>(vl, sa, st * 4096, sl + 20480 + l * 4096); bdma_m0<1024>(vl, sa, st * 4096); bdma_m0<2048>(vl, sa, st * 4096); bdma_m0<3072>(vl, sa, st * 4096);
        };
        issue(0); issue(1); issue(2);
        for (int st = 0; st <= P3_NST; ++st) {
            WAIT_VM(18);
            BAR();
            issue(st + 3);
        }
        WAIT_VM(0);
        return;
    }
    f32x4 acc[5];
#pragma unroll
    for (int i = 0; i < 5; ++i) acc[i] = (f32x4){0.f, 0.f, 0.f, 0.f};
    bf16x8 aa[2], wa[2][5], ab[2], wb[2][5];
    const unsigned wbase = lds0 + vl, abase = lds0 + 20480 + wid * 4096 + vl;
#define RDA(dst, k) dst = *(const LAS bf16x8*)(size_t)(as + (k) * 1024)
#define RDW(dst, k, ng) dst = *(const LAS bf16x8*)(size_t)(wsb + (k) * 5120 + (ng) * 1024)
#define PHASE(af, wf, na, nw, hf) do { \
        _Pragma("unroll") for (int k = 0; k < 2; ++k) { \
            acc[0] = MFMA16(wf[k][0], af[k], acc[0]); SBAR(); RDA(na[k], (hf) * 2 + k); SBAR(); \
            acc[1] = MFMA16(wf[k][1], af[k], acc[1]); SBAR(); RDW(nw[k][0], (hf) * 2 + k, 0); SBAR(); \
            acc[2] = MFMA16(wf[k][2], af[k], acc[2]); SBAR(); RDW(nw[k][1], (hf) * 2 + k, 1); RDW(nw[k][2], (hf) * 2 + k, 2); SBAR(); \
            acc[3] = MFMA16(wf[k][3], af[k], acc[3]); SBAR(); RDW(nw[k][3], (hf) * 2 + k, 3); SBAR(); \
            acc[4] = MFMA16(wf[k][4], af[k], acc[4]); SBAR(); RDW(nw[k][4], (hf) * 2 + k, 4); SBAR(); \
        } } while (0)
    ST(1);
    BAR();
    ST(2);
    { const unsigned as = abase, wsb = wbase;
#pragma unroll
      for (int k = 0; k < 2; ++k) { RDA(aa[k], k);
#pragma unroll
        for (int ng = 0; ng < 5; ++ng) RDW(wa[k][ng], k, ng); } }
    for (int st = 0; st < P3_NST; ++st) {
        { const unsigned so = (st & 3) * P3_SLOT; const unsigned as = abase + so, wsb = wbase + so; PHASE(aa, wa, ab, wb, 1); }
        WAIT_LGKM0();
        BAR();
        if (st == 4) ST(3);
        if (st == 12) ST(4);
        { const unsigned so = ((st + 1) & 3) * P3_SLOT; const unsigned as = abase + so, wsb = wbase + so; PHASE(ab, wb, aa, wa, 0); }
    }
#undef PHASE
#undef RDA
#undef RDW
    ST(5);
    const int tok = mt * 64 + wid * 16 + (lane & 15), rq = lane >> 4;
    const size_t off = (size_t)tok * D_ + nt * 80 + rq * 20;
#pragma unroll
    for (int ng = 0; ng < 5; ++ng) {
        const u32x2 xr = *(const u32x2*)(x + off + ng * 4);
        const float r0 = __uint_as_float(xr.x << 16) + acc[ng][0], r1 = __uint_as_float(xr.x & 0xffff0000u) + acc[ng][1];
        const float r2 = __uint_as_float(xr.y << 16) + acc[ng][2], r3 = __uint_as_float(xr.y & 0xffff0000u) + acc[ng][3];
        *(u32x2*)(out + off + ng * 4) = (u32x2){pk2(r0, r1), pk2(r2, r3)};
    }
    ST(6);
    WAIT_VM(0);
    ST(7);
    ST_OUT(3);
}

extern "C" void kernel_launch(void* const* d_in, const int* in_sizes, int n_in, void* d_out, int out_size, void* d_ws, size_t ws_size, hipStream_t stream) {
    const u16* x = (const u16*)d_in[0];
    const char* wqkv = (const char*)d_in[1];
    const u16* wo = (const u16*)d_in[2];
    const char* wg = (const char*)d_in[4];
    const char* wu = (const char*)d_in[5];
    const u16* wd = (const u16*)d_in[6];
    char* ws = (char*)d_ws;
    (void)hipFuncSetAttribute((const void*)p1_k, hipFuncAttributeMaxDynamicSharedMemorySize, P1_SLOT * P1_NSLOT);
    (void)hipFuncSetAttribute((const void*)p3_k, hipFuncAttributeMaxDynamicSharedMemorySize, P3_SLOT * P3_NSLOT);
    prep_k<<<256 + 148, 256, 0, stream>>>(x, wo, wd, ws);
    p1_k<<<256, 768, P1_SLOT * P1_NSLOT, stream>>>(wqkv, wg, wu, ws);
    attn_k<<<256, 512, AT_LDS, stream>>>(ws);
    p3_k<<<256, 512, P3_SLOT * P3_NSLOT, stream>>>(x, ws, (u16*)d_out);
}
extern "C" void launch_one(int which, void* const* d_in, void* d_out, void* d_ws, hipStream_t stream) {
    const u16* x = (const u16*)d_in[0];
    const char* wqkv = (const char*)d_in[1];
    const u16* wo = (const u16*)d_in[2];
    const char* wg = (const char*)d_in[4];
    const char* wu = (const char*)d_in[5];
    const u16* wd = (const u16*)d_in[6];
    char* ws = (char*)d_ws;
    if (which == 0) prep_k<<<256 + 148, 256, 0, stream>>>(x, wo, wd, ws);
    if (which == 1) p1_k<<<256, 768, P1_SLOT * P1_NSLOT, stream>>>(wqkv, wg, wu, ws);
    if (which == 2) attn_k<<<256, 512, AT_LDS, stream>>>(ws);
    if (which == 3) p3_k<<<256, 512, P3_SLOT * P3_NSLOT, stream>>>(x, ws, (u16*)d_out);
}
```

```cpp
#include <hip/hip_runtime.h>
#include <hip/hip_bf16.h>
#include <stdint.h>

typedef unsigned short u16;
typedef short bf16x8 __attribute__((ext_vector_type(8)));
typedef short s16x4 __attribute__((ext_vector_type(4)));
typedef float f32x16 __attribute__((ext_vector_type(16)));
typedef float f32x4 __attribute__((ext_vector_type(4)));
typedef float f32x2 __attribute__((ext_vector_type(2)));
typedef unsigned u32x4 __attribute__((ext_vector_type(4)));
typedef unsigned u32x2 __attribute__((ext_vector_type(2)));
typedef __bf16 bf16x2_t __attribute__((ext_vector_type(2)));
typedef int srd_t __attribute__((ext_vector_type(4)));
typedef short v4i16_t __attribute__((ext_vector_type(4)));
#define LAS __attribute__((address_space(3)))
#define GAS __attribute__((address_space(1)))
#define DI __device__ __forceinline__
typedef LAS const char* lds_cptr;

constexpr int D_ = 640, DH = 20;
constexpr int NKS3 = 74;
constexpr unsigned WS_FLAG = 0;
constexpr unsigned FL_E = 0, FL_TMO = 128, FL_S = 1024, FL_Q = 4096, FL_H = FL_Q + 8192, FL_O = FL_H + 8192, FL_W = FL_O + 8192;
constexpr unsigned WS_QKV = 65536;
constexpr unsigned WS_ACT = WS_QKV + 480u * 16384u;
constexpr unsigned WS_WT3 = WS_ACT + 128u * NKS3 * 1024u;
constexpr unsigned WS_END = WS_WT3 + 8u * NKS3 * 5u * 1024u;
constexpr int L_STAT = 98304, L_CS = L_STAT + 4096;
constexpr int AT_PL = 4096 + 32;
constexpr int L_K = 110592, L_V = L_K + 5 * AT_PL, L_ONES = L_K + 10 * AT_PL, L_QP = L_ONES + 64, L_TAG = L_QP + 10240, L_CSG = L_TAG + 16, LDS_BYTES = 163840;
static_assert(L_CSG + 1024 <= LDS_BYTES && (L_CSG & 15) == 0, "lds");
constexpr int P3_SLOT = 36864, P3_NST = 19;

DI unsigned pk2(float a, float b) { f32x2 v = {a, b}; bf16x2_t r = __builtin_convertvector(v, bf16x2_t); return __builtin_bit_cast(unsigned, r); }
DI s16x4 vtr(unsigned a) { return __builtin_bit_cast(s16x4, __builtin_amdgcn_ds_read_tr16_b64_v4i16((LAS v4i16_t*)(size_t)a)); }
DI bf16x8 cat8(s16x4 a, s16x4 b) { return __builtin_shufflevector(a, b, 0, 1, 2, 3, 4, 5, 6, 7); }
DI bf16x8 ldsr128(unsigned a) { return *(const LAS bf16x8*)(size_t)a; }
DI float dot2(unsigned a, unsigned b, float c) { return __builtin_amdgcn_fdot2_f32_bf16(__builtin_bit_cast(bf16x2_t, a), __builtin_bit_cast(bf16x2_t, b), c, false); }
DI srd_t make_srd(const void* base, unsigned bytes) {
    const uint64_t v = (uint64_t)base;
    srd_t r; r[0] = (int)(unsigned)v; r[1] = (int)((unsigned)(v >> 32) & 0xffffu); r[2] = (int)bytes; r[3] = 0x00020000; return r;
}
template <int OFF, int SC> DI void bdma(unsigned voff, srd_t srd, unsigned soff, unsigned dst) {
    if (SC) asm volatile("s_mov_b32 m0, %3\n\ts_nop 0\n\tbuffer_load_dwordx4 %0, %1, %2 offen offset:%4 sc1 lds" ::"v"(voff), "s"(srd), "s"(soff), "s"(dst), "n"(OFF) : "memory");
    else asm volatile("s_mov_b32 m0, %3\n\ts_nop 0\n\tbuffer_load_dwordx4 %0, %1, %2 offen offset:%4 lds" ::"v"(voff), "s"(srd), "s"(soff), "s"(dst), "n"(OFF) : "memory");
}
template <int OFF, int SC> DI void bdma_m0(unsigned voff, srd_t srd, unsigned soff) {
    if (SC) asm volatile("buffer_load_dwordx4 %0, %1, %2 offen offset:%3 sc1 lds" ::"v"(voff), "s"(srd), "s"(soff), "n"(OFF) : "memory");
    else asm volatile("buffer_load_dwordx4 %0, %1, %2 offen offset:%3 lds" ::"v"(voff), "s"(srd), "s"(soff), "n"(OFF) : "memory");
}
#define WAIT_VM(n) asm volatile("s_waitcnt vmcnt(%0)" ::"n"(n) : "memory")
#define WAIT_LGKM0() asm volatile("s_waitcnt lgkmcnt(0)" ::: "memory")
#define BAR() do { asm volatile("" ::: "memory"); __builtin_amdgcn_s_barrier(); asm volatile("" ::: "memory"); } while (0)
#define SBAR() __builtin_amdgcn_sched_barrier(0)
#define MFMA32(a, b, c) __builtin_amdgcn_mfma_f32_32x32x16_bf16((a), (b), (c), 0, 0, 0)
#define MFMA16(a, b, c) __builtin_amdgcn_mfma_f32_16x16x32_bf16((a), (b), (c), 0, 0, 0)
#define RLX_AGENT __ATOMIC_RELAXED, __HIP_MEMORY_SCOPE_AGENT
typedef GAS unsigned gu32;

#define ST(i)
#define ST_DECL
#define ST_OUT(kid)

DI void st64(__amdgpu_buffer_rsrc_t rs, unsigned off, u32x2 v) { __builtin_amdgcn_raw_buffer_store_b64(v, rs, off, 0, 16); }
DI void st128(__amdgpu_buffer_rsrc_t rs, unsigned off, u32x4 v) { __builtin_amdgcn_raw_buffer_store_b128(v, rs, off, 0, 16); }
DI void set_flag(char* ws, unsigned off, unsigned tag, int lane) {
    WAIT_VM(0);
    if (lane == 0) __hip_atomic_store((gu32*)(ws + off), tag, RLX_AGENT);
}
template <int N> DI void poll_flags(char* ws, unsigned off, bool act, unsigned tag) {
    for (unsigned spins = 0;; ++spins) {
        bool ok = true;
        if (act) {
#pragma unroll
            for (int i = 0; i < N; ++i) ok &= (__hip_atomic_load((gu32*)(ws + off) + i, RLX_AGENT) == tag);
        }
        if (__all(ok)) break;
        if (spins > (1u << 16)) { __hip_atomic_store((gu32*)(ws + FL_TMO), 1u, RLX_AGENT); break; }
        __builtin_amdgcn_s_sleep(2);
    }
}

__global__ __launch_bounds__(768, 3) void mega_k(const char* __restrict__ x, const char* __restrict__ wqkv, const char* __restrict__ wo, const char* __restrict__ wg,
                                                 const char* __restrict__ wu, const char* __restrict__ wd, char* __restrict__ ws, char* __restrict__ out) {
    extern __shared__ __attribute__((aligned(16))) char lds[];
    ST_DECL ST(0);
    const int tid = threadIdx.x, lane = tid & 63, wid = __builtin_amdgcn_readfirstlane(tid >> 6);
    const int b = blockIdx.x, xcd = b & 7, idx = b >> 3;
    const int panel = (xcd & 1) * 8 + (idx & 7), c = (xcd >> 1) * 4 + (idx >> 3);
    const int qb = xcd, head = idx;
    const int mt = xcd * 4 + (idx & 3), nt = idx >> 2;
    const unsigned lds0 = (unsigned)(size_t)lds;
    const bool hasQ = c < 15, hasG = c < 14;
    const int nret = c == 15 ? 3 : (c == 14 ? (64 + panel < NKS3 ? 2 : 1) : 0);
    const int ret0 = c == 15 ? panel : 48 + panel;

    if (wid >= 8) {
        const int l = wid - 8;
        unsigned tag0 = 0;
        if (l == 0) {
            const unsigned e = __hip_atomic_load((gu32*)(ws + FL_E), RLX_AGENT);
            tag0 = __builtin_amdgcn_readfirstlane(e) + 1u;
            if (lane == 0) *(LAS unsigned*)(size_t)(lds0 + L_TAG) = tag0;
        }
        auto get_tag = [&]() -> unsigned { return __builtin_amdgcn_readfirstlane(*(const volatile LAS unsigned*)(size_t)(lds0 + L_TAG)); };
        const int r8 = lane >> 3, cc = lane & 7;
        const unsigned csw = (cc ^ (((r8 >> 1) & 1) << 2)) * 16;
        const unsigned vq = r8 * 3840 + csw, vg = r8 * 3456 + csw;
        const unsigned vx0 = r8 * 1280 + ((cc ^ (r8 >> 1)) * 16), vx1 = r8 * 1280 + ((cc ^ (4 + (r8 >> 1))) * 16);
        const unsigned vl = lane * 16;
        const srd_t sx = make_srd(x + (unsigned)(panel * 128 * 1280), 128 * 1280);
        auto issue_x = [&](int kb, unsigned dst) {
#pragma unroll
            for (int i = 0; i < 4; ++i) bdma<0, 0>((i & 1) ? vx1 : vx0, sx, (4 * l + i) * 10240 + kb * 128, dst + (4 * l + i) * 1024);
        };
        auto issue_ret = [&](int u, unsigned dst) {
            const srd_t sr = u < 54 ? make_srd(wd, 1728u * 1280u) : make_srd(wo, 640u * 1280u);
#pragma unroll
            for (int i = 0; i < 10; ++i) {
                const int ci = (l * 10 + i) * 64 + lane, sr_ = ci / 80, ch = ci - sr_ * 80;
                const int kg = sr_ >> 3, j = sr_ & 7;
                int row;
                if (u < 54) row = 32 * u + 16 * (kg >> 1) + 8 * (j >> 2) + 4 * (kg & 1) + (j & 3);
                else {
                    const int slot = 4 * (u - 54) + kg, a = slot / 5, ii = slot - 5 * a;
                    row = ii < 4 ? (2 * a + (ii >> 1)) * DH + (ii & 1) * 4 + (j & 3) + 8 * (j >> 2) : (2 * a + (j >> 2)) * DH + 16 + (j & 3);
                }
                bdma<0, 0>(row * 1280 + ch * 16, sr, 0, dst + (l * 10 + i) * 1024);
            }
        };
        if (c == 15) { issue_ret(ret0, lds0); issue_ret(ret0 + 16, lds0 + 40960); issue_ret(ret0 + 32, lds0 + 81920); }
        if (l == 0 && lane == 0) __hip_atomic_store((gu32*)(ws + FL_S) + b, tag0, RLX_AGENT);
        if (hasQ) {
            const srd_t sw = make_srd(wqkv + c * 256, 640u * 3840u - c * 256);
            auto issue = [&](int st) {
                const unsigned sl = lds0 + (st & 3) * 32768;
                issue_x(st, sl);
#pragma unroll
                for (int i = 0; i < 4; ++i) { const int j = 4 * l + i; bdma<0, 0>(vq, sw, (st * 64 + 8 * (j & 7)) * 3840 + (j >> 3) * 128, sl + 16384 + j * 1024); }
            };
            issue(0);
            __builtin_amdgcn_s_sleep(4);
            issue(1); issue(2);
            for (int st = 0; st <= 10; ++st) {
                if (st <= 7) { WAIT_VM(16); } else if (st == 8) { WAIT_VM(8); } else { WAIT_VM(0); }
                WAIT_LGKM0();
                BAR();
                if (st + 3 < 10) issue(st + 3);
            }
        }
        auto issue_att = [&]() {
            const unsigned tag = get_tag();
            {
                const int part = lane / 16, rem = lane & 15, cand = rem >> 3, pn = (rem >> 1) & 3, hf = rem & 1;
                const int f0 = part * 640 + head * DH + (cand ? DH - 1 : 0);
                int pp = 2 * qb - 2 + pn; if (pp < 0) pp = 0;
                poll_flags<4>(ws, FL_Q + ((pp * 16 + (f0 >> 7)) * 8 + hf * 4) * 4, lane < 48, tag);
            }
            const srd_t sq = make_srd(ws + WS_QKV, 480u * 16384u);
            const int kb0 = 256 * qb - 256;
#pragma unroll
            for (int i = 0; i < 10; ++i) {
                const int pc = l * 10 + i, pl = pc >> 2, qu = pc & 3;
                int key0 = kb0 + qu * 128; if (key0 < 0) key0 += 256;
                const int gp = (pl < 5 ? 160 : 315) + head * 5 + pl;
                bdma<0, 1>(vl, sq, gp * 16384 + key0 * 8, lds0 + L_K + pl * AT_PL + qu * 1024);
            }
#pragma unroll
            for (int i = 0; i < 3; ++i) {
                const int pc = l + 4 * i;
                if (pc < 10) bdma<0, 1>(vl, sq, (head * 5 + (pc >> 1)) * 16384 + (256 * qb + (pc & 1) * 128) * 8, lds0 + L_QP + pc * 1024);
            }
        };
        if (hasG) {
            const int np = c < 13 ? 2 : 1;
            const srd_t s0 = make_srd(wg + c * 256, 640u * 3456u - c * 256), s1 = make_srd(wu + c * 256, 640u * 3456u - c * 256);
            const srd_t s2 = make_srd(wg + c * 256 + 128, np == 2 ? 640u * 3456u - c * 256 - 128 : 0u), s3 = make_srd(wu + c * 256 + 128, np == 2 ? 640u * 3456u - c * 256 - 128 : 0u);
            auto issue_w = [&](int st) {
                const unsigned dw = lds0 + (st & 3) * 16384 + l * 1024, so = (st * 32 + l * 8) * 3456;
                bdma<0, 0>(vg, s0, so, dw); bdma<0, 0>(vg, s1, so, dw + 4096); bdma<0, 0>(vg, s2, so, dw + 8192); bdma<0, 0>(vg, s3, so, dw + 12288);
            };
            issue_x(0, lds0 + 65536); issue_w(0);
            issue_w(1); issue_x(1, lds0 + 65536 + 16384); issue_w(2);
            if (hasQ) BAR();
            for (int st = 0; st <= 20; ++st) {
                if (st == 0) { WAIT_VM(12); } else if (st == 1) { WAIT_VM(12); }
                else if (st == 13 || st == 14 || st == 15) { }
                else if (st >= 18) { WAIT_VM(0); } else if (st & 1) { WAIT_VM(12); } else { WAIT_VM(8); }
                BAR();
                if (!(st & 1) && st >= 2 && (st >> 1) + 1 < 10) issue_x((st >> 1) + 1, lds0 + 65536 + (((st >> 1) + 1) & 1) * 16384);
                if (st + 3 < 20) issue_w(st + 3);
                if (st == 12) issue_att();
            }
        } else {
            if (hasQ) BAR();
            if (c == 14) { issue_ret(ret0, lds0); if (nret > 1) issue_ret(ret0 + 16, lds0 + 40960); }
            WAIT_VM(0);
            WAIT_LGKM0();
            BAR();
            BAR();
            issue_att();
        }
        WAIT_VM(0);
        BAR();
        ST(1);
        const unsigned tag = get_tag();
        if (b == 0 && l == 1) { poll_flags<4>(ws, FL_S + lane * 16, true, tag); if (lane == 0) __hip_atomic_store((gu32*)(ws + FL_E), tag, RLX_AGENT); }
        {
            poll_flags<4>(ws, FL_H + (((mt >> 1) * 16) * 8) * 4 + lane * 16, lane < 28, tag);
            poll_flags<2>(ws, FL_W + (nt * 80 + lane * 2) * 4, lane < 37, tag);
            ST(2);
            const srd_t sa = make_srd(ws + WS_ACT + (unsigned)((mt * 4 + l) * NKS3 * 1024), NKS3 * 1024);
            const srd_t sw = make_srd(ws + WS_WT3 + (unsigned)(nt * NKS3 * 5 * 1024), NKS3 * 5 * 1024);
            auto issue = [&](int st) {
                const unsigned sl = lds0 + (st & 3) * P3_SLOT, ow = st * 20480 + l * 5120;
                bdma<0, 1>(vl, sw, ow, sl + l * 5120); bdma_m0<1024, 1>(vl, sw, ow); bdma_m0<2048, 1>(vl, sw, ow); bdma_m0<3072, 1>(vl, sw, ow);
                bdma<0, 1>(vl, sw, ow + 4096, sl + l * 5120 + 4096);
                bdma<0, 1>(vl, sa, st * 4096, sl + 20480 + l * 4096); bdma_m0<1024, 1>(vl, sa, st * 4096); bdma_m0<2048, 1>(vl, sa, st * 4096); bdma_m0<3072, 1>(vl, sa, st * 4096);
            };
            issue(0); issue(1); issue(2);
            for (int st = 0; st <= P3_NST; ++st) {
                if (st + 2 < P3_NST) { WAIT_VM(18); } else if (st + 1 < P3_NST) { WAIT_VM(9); } else { WAIT_VM(0); }
                BAR();
                if (st == 10) poll_flags<1>(ws, FL_O + ((qb * 8 + 2 * (mt & 3)) * 32 + lane) * 4, true, tag);
                if (st + 3 < P3_NST) issue(st + 3);
            }
        }
        ST(3);
        ST_OUT(4 + l);
        return;
    }

    const int cl = lane & 31, h = lane >> 5;
    const int g = lane >> 4, q = (lane & 15) >> 2, p = lane & 3;
    const int th = wid & 1;
    const __amdgpu_buffer_rsrc_t rws = __builtin_amdgcn_make_buffer_rsrc(ws, 0, WS_END, 0x00020000);
    unsigned xad[4];
#pragma unroll
    for (int ks = 0; ks < 4; ++ks) xad[ks] = lds0 + (64 * th + cl) * 128 + (((2 * ks + h) ^ ((cl >> 1) & 7)) * 16);
    const unsigned wlane = (8 * (g >> 1) + q) * 128 + (p & 1) * 8;
    unsigned tag = 0;
    float mean[2] = {0.f, 0.f}, rstd[2] = {0.f, 0.f};
    const unsigned ONES = 0x3f803f80u;
    if (tid < 2) *(LAS u32x2*)(size_t)(lds0 + L_ONES + tid * 8) = (u32x2){0x00003f80u, 0u};

    if (hasQ) {
        const int blk = wid >> 1, unit = blk >> 1, fh = blk & 1;
        const unsigned wad = lds0 + 16384 + unit * 8192 + wlane + (((fh * 4 + (g & 1) * 2 + (p >> 1)) ^ (((q >> 1) & 1) << 2)) * 16);
        f32x16 acc[2];
#pragma unroll
        for (int r = 0; r < 16; ++r) { acc[0][r] = 0.f; acc[1][r] = 0.f; }
        float ssum[2] = {0.f, 0.f}, ssq[2] = {0.f, 0.f}, csum = 0.f;
        bf16x8 xa[2][2], wa[2], xb[2][2], wb[2];
#define Q_RD(xx, ww, so, k0) do { _Pragma("unroll") for (int k = 0; k < 2; ++k) { xx[k][0] = ldsr128(xad[(k0) + k] + (so)); xx[k][1] = ldsr128(xad[(k0) + k] + (so) + 4096); \
            ww[k] = cat8(vtr(wad + (so) + ((k0) + k) * 2048), vtr(wad + (so) + ((k0) + k) * 2048 + 512)); } } while (0)
#define Q_STAT(xx, ww, k0) do { _Pragma("unroll") for (int k = 0; k < 2; ++k) { \
            if (blk == (k0) + k) { _Pragma("unroll") for (int t = 0; t < 2; ++t) { const u32x4 v = __builtin_bit_cast(u32x4, xx[k][t]); \
                _Pragma("unroll") for (int j = 0; j < 4; ++j) { ssum[t] = dot2(v[j], ONES, ssum[t]); ssq[t] = dot2(v[j], v[j], ssq[t]); } } } \
            if (th == ((k0) >> 1)) { const u32x4 v = __builtin_bit_cast(u32x4, ww[k]); _Pragma("unroll") for (int j = 0; j < 4; ++j) csum = dot2(v[j], ONES, csum); } } } while (0)
#define Q_MM(xx, ww) do { _Pragma("unroll") for (int k = 0; k < 2; ++k) { acc[0] = MFMA32(ww[k], xx[k][0], acc[0]); acc[1] = MFMA32(ww[k], xx[k][1], acc[1]); } } while (0)
        BAR();
        tag = *(const LAS unsigned*)(size_t)(lds0 + L_TAG);
        Q_RD(xa, wa, 0, 0);
        for (int st = 0; st < 10; ++st) {
            const unsigned so = (st & 3) * 32768, sn = ((st + 1) & 3) * 32768;
            Q_RD(xb, wb, so, 2);
            SBAR();
            Q_MM(xa, wa); Q_STAT(xa, wa, 0);
            SBAR();
            WAIT_LGKM0();
            BAR();
            if (st < 9) Q_RD(xa, wa, sn, 0);
            SBAR();
            Q_MM(xb, wb); Q_STAT(xb, wb, 2);
            SBAR();
        }
#undef Q_RD
#undef Q_STAT
#undef Q_MM
        {
#pragma unroll
            for (int t = 0; t < 2; ++t) {
                auto a = __builtin_amdgcn_permlane32_swap(__float_as_uint(ssum[t]), __float_as_uint(ssum[t]), false, false);
                auto bb = __builtin_amdgcn_permlane32_swap(__float_as_uint(ssq[t]), __float_as_uint(ssq[t]), false, false);
                const float s = __uint_as_float(a[0]) + __uint_as_float(a[1]), qq = __uint_as_float(bb[0]) + __uint_as_float(bb[1]);
                if (h == 0) *(LAS f32x2*)(size_t)(lds0 + L_STAT + (blk * 128 + 64 * th + 32 * t + cl) * 8) = (f32x2){s, qq};
            }
            auto a = __builtin_amdgcn_permlane32_swap(__float_as_uint(csum), __float_as_uint(csum), false, false);
            const float s = __uint_as_float(a[0]) + __uint_as_float(a[1]);
            if (h == 0) *(LAS float*)(size_t)(lds0 + L_CS + (th * 256 + blk * 32 + cl) * 4) = s;
        }
        WAIT_LGKM0();
        BAR();
#pragma unroll
        for (int t = 0; t < 2; ++t) {
            float s = 0.f, qq = 0.f;
#pragma unroll
            for (int w = 0; w < 4; ++w) { const f32x2 v = *(const LAS f32x2*)(size_t)(lds0 + L_STAT + (w * 128 + 64 * th + 32 * t + cl) * 8); s += v[0]; qq += v[1]; }
            mean[t] = s * (1.f / D_);
            rstd[t] = __builtin_amdgcn_rsqf(__builtin_fmaxf(qq * (1.f / D_) - mean[t] * mean[t], 0.f) + 1e-5f);
        }
        {
            const int f40 = (2 * c + unit) * 16 + fh * 8 + h;
            const float sc = (2 * c + unit < 10) ? 0.22360679774997896f * 1.4426950408889634f : 1.f;
            f32x4 cs[4];
#pragma unroll
            for (int gg = 0; gg < 4; ++gg) cs[gg] = *(const LAS f32x4*)(size_t)(lds0 + L_CS + (blk * 32 + 8 * gg + 4 * h) * 4) + *(const LAS f32x4*)(size_t)(lds0 + L_CS + (256 + blk * 32 + 8 * gg + 4 * h) * 4);
#pragma unroll
            for (int t = 0; t < 2; ++t) {
                const int tok = panel * 128 + th * 64 + t * 32 + cl;
                const float rs = rstd[t] * sc, nm = -mean[t];
#pragma unroll
                for (int gg = 0; gg < 4; ++gg) {
                    float v[4];
#pragma unroll
                    for (int j = 0; j < 4; ++j) v[j] = __builtin_fmaf(nm, cs[gg][j], acc[t][4 * gg + j]) * rs;
                    st64(rws, WS_QKV + (unsigned)(f40 + 2 * gg) * 16384u + tok * 8, (u32x2){pk2(v[0], v[1]), pk2(v[2], v[3])});
                }
            }
        }
        if (!hasG) set_flag(ws, FL_Q + ((panel * 16 + c) * 8 + wid) * 4, tag, lane);
    }
    ST(1);
    if (hasG) {
        const int fh = (wid >> 1) & 1, pr = wid >> 2;
        const bool act = (c < 13) || pr == 0;
        const unsigned wad = lds0 + pr * 8192 + wlane + (((fh * 4 + (g & 1) * 2 + (p >> 1)) ^ (((q >> 1) & 1) << 2)) * 16);
        f32x16 acc[2][2];
#pragma unroll
        for (int r = 0; r < 16; ++r) { acc[0][0][r] = 0.f; acc[0][1][r] = 0.f; acc[1][0][r] = 0.f; acc[1][1][r] = 0.f; }
        float csg = 0.f, csu = 0.f;
        bf16x8 xa[2], wa[2], xb[2], wb[2];
#define G_PHASE(xf, wf, nx, nw, so_w, ks, so_x, kx) do { \
            acc[0][0] = MFMA32(wf[0], xf[0], acc[0][0]); SBAR(); nx[0] = ldsr128(xad[kx] + (so_x)); nx[1] = ldsr128(xad[kx] + (so_x) + 4096); SBAR(); \
            acc[1][0] = MFMA32(wf[0], xf[1], acc[1][0]); SBAR(); nw[0] = cat8(vtr(wad + (so_w) + (ks) * 2048), vtr(wad + (so_w) + (ks) * 2048 + 512)); SBAR(); \
            acc[0][1] = MFMA32(wf[1], xf[0], acc[0][1]); SBAR(); nw[1] = cat8(vtr(wad + (so_w) + 4096 + (ks) * 2048), vtr(wad + (so_w) + 4096 + (ks) * 2048 + 512)); SBAR(); \
            acc[1][1] = MFMA32(wf[1], xf[1], acc[1][1]); \
            { const u32x4 v0 = __builtin_bit_cast(u32x4, wf[0]), v1 = __builtin_bit_cast(u32x4, wf[1]); \
                _Pragma("unroll") for (int j = 0; j < 4; ++j) { csg = dot2(v0[j], ONES, csg); csu = dot2(v1[j], ONES, csu); } } \
            SBAR(); } while (0)
        BAR();
        xa[0] = ldsr128(xad[0] + 65536); xa[1] = ldsr128(xad[0] + 65536 + 4096);
        wa[0] = cat8(vtr(wad), vtr(wad + 512)); wa[1] = cat8(vtr(wad + 4096), vtr(wad + 4096 + 512));
        for (int s2 = 0; s2 < 10; ++s2) {
            const unsigned sx0 = 65536 + (s2 & 1) * 16384, sx1 = 65536 + ((s2 + 1) & 1) * 16384;
            const unsigned w0 = ((2 * s2) & 3) * 16384, w1 = ((2 * s2 + 1) & 3) * 16384, w2 = ((2 * s2 + 2) & 3) * 16384;
            if (act) G_PHASE(xa, wa, xb, wb, w0, 1, sx0, 1);
            WAIT_LGKM0();
            BAR();
            if (act) G_PHASE(xb, wb, xa, wa, w1, 0, sx0, 2);
            if (act) G_PHASE(xa, wa, xb, wb, w1, 1, sx0, 3);
            if (s2 == 1) set_flag(ws, FL_Q + ((panel * 16 + c) * 8 + wid) * 4, tag, lane);
            WAIT_LGKM0();
            BAR();
            if (act) G_PHASE(xb, wb, xa, wa, w2, 0, sx1, 0);
        }
#undef G_PHASE
        const unsigned csa = lds0 + L_CSG + (pr * 2 + fh) * 256;
        {
            auto a = __builtin_amdgcn_permlane32_swap(__float_as_uint(csg), __float_as_uint(csg), false, false);
            auto bb = __builtin_amdgcn_permlane32_swap(__float_as_uint(csu), __float_as_uint(csu), false, false);
            *(LAS float*)(size_t)(csa + h * 128 + cl * 4) = h ? __uint_as_float(bb[0]) + __uint_as_float(bb[1]) : __uint_as_float(a[0]) + __uint_as_float(a[1]);
        }
        if (act) {
            const int ks3 = (2 * c + pr) * 2 + fh;
#pragma unroll
            for (int t = 0; t < 2; ++t) {
                const int tok = panel * 128 + th * 64 + t * 32 + cl;
                const float rs2 = rstd[t] * rstd[t], nm = -mean[t];
#pragma unroll
                for (int gp = 0; gp < 2; ++gp) {
                    float hv[8];
#pragma unroll
                    for (int gq = 0; gq < 2; ++gq) {
                        const int gg = 2 * gp + gq;
                        const unsigned ca = csa + (8 * gg + 4 * h) * 4;
                        const f32x4 cg = *(const LAS f32x4*)(size_t)(ca), cu = *(const LAS f32x4*)(size_t)(ca + 128);
#pragma unroll
                        for (int j = 0; j < 4; ++j)
                            hv[4 * gq + j] = __builtin_fmaf(nm, cg[j], acc[t][0][4 * gg + j]) * __builtin_fmaf(nm, cu[j], acc[t][1][4 * gg + j]) * rs2;
                    }
                    st128(rws, WS_ACT + (unsigned)((tok >> 4) * NKS3 + ks3) * 1024u + ((tok & 15) + 16 * (2 * gp + h)) * 16,
                          (u32x4){pk2(hv[0], hv[1]), pk2(hv[2], hv[3]), pk2(hv[4], hv[5]), pk2(hv[6], hv[7])});
                }
            }
        }
        BAR();
    } else {
        BAR();
        if (!hasQ) tag = *(const LAS unsigned*)(size_t)(lds0 + L_TAG);
        for (int i = 0; i < nret; ++i) {
            const int u = ret0 + 16 * i;
            const unsigned ra = lds0 + i * 40960 + (8 * g + q) * 1280 + 160 * wid + 40 * p;
#pragma unroll
            for (int ng = 0; ng < 5; ++ng) {
                const bf16x8 f = cat8(vtr(ra + 8 * ng), vtr(ra + 8 * ng + 4 * 1280));
                st128(rws, WS_WT3 + (unsigned)((wid * NKS3 + u) * 5 + ng) * 1024u + lane * 16, __builtin_bit_cast(u32x4, f));
            }
        }
        WAIT_VM(0);
        if (lane < nret) __hip_atomic_store((gu32*)(ws + FL_W) + wid * 80 + ret0 + 16 * lane, tag, RLX_AGENT);
        WAIT_LGKM0();
        BAR();
        BAR();
    }
    ST(2);
    {
        bf16x8 qf0, qf1;
        {
            const unsigned qp = lds0 + L_QP + (32 * wid + cl) * 8;
            const u32x2 a = *(const LAS u32x2*)(size_t)(qp + (2 * h) * 2048), bq = *(const LAS u32x2*)(size_t)(qp + (2 * h + 1) * 2048), e = *(const LAS u32x2*)(size_t)(qp + 4 * 2048);
            qf0 = __builtin_bit_cast(bf16x8, (u32x4){a.x, a.y, bq.x, bq.y});
            qf1 = __builtin_bit_cast(bf16x8, (u32x4){h ? 0u : e.x, h ? 0u : e.y, 0u, 0u});
        }
        f32x16 s[9];
        const unsigned kp = lds0 + L_K + (2 * h) * AT_PL + (32 * wid + cl) * 8, kp4 = lds0 + L_K + 4 * AT_PL + (32 * wid + cl) * 8;
        f32x16 zero;
#pragma unroll
        for (int r = 0; r < 16; ++r) zero[r] = 0.f;
#pragma unroll
        for (int j = 0; j < 9; ++j) {
            const u32x2 a = *(const LAS u32x2*)(size_t)(kp + j * 256), bq = *(const LAS u32x2*)(size_t)(kp + AT_PL + j * 256), e = *(const LAS u32x2*)(size_t)(kp4 + j * 256);
            s[j] = MFMA32(__builtin_bit_cast(bf16x8, (u32x4){a.x, a.y, bq.x, bq.y}), qf0, zero);
            s[j] = MFMA32(__builtin_bit_cast(bf16x8, (u32x4){e.x, e.y, e.x, e.y}), qf1, s[j]);
        }
        const float NEG = -__builtin_inff();
        const int ch = cl - 4 * h;
#pragma unroll
        for (int r = 0; r < 16; ++r) {
            const int rr = (r & 3) + 8 * (r >> 2);
            if (!(rr > ch)) s[0][r] = NEG;
            if (rr > ch) s[8][r] = NEG;
        }
        if (qb == 0) {
#pragma unroll
            for (int j = 0; j < 8; ++j)
                if (j < 8 - wid) {
#pragma unroll
                    for (int r = 0; r < 16; ++r) s[j][r] = NEG;
                }
        }
        float m = NEG;
#pragma unroll
        for (int j = 0; j < 9; ++j)
#pragma unroll
            for (int r = 0; r < 16; r += 2) m = __builtin_fmaxf(__builtin_fmaxf(m, s[j][r]), s[j][r + 1]);
        { auto rr = __builtin_amdgcn_permlane32_swap(__float_as_uint(m), __float_as_uint(m), false, false);
          m = __builtin_fmaxf(__uint_as_float(rr[0]), __uint_as_float(rr[1])); }
        if (hasG) set_flag(ws, FL_H + ((panel * 16 + c) * 8 + wid) * 4, tag, lane);
        const int dq = 4 * (g & 1) + p;
        const bool real = dq < 5;
        const unsigned vbase = real ? lds0 + L_V + dq * AT_PL + (32 * wid + 4 * h + q) * 8 : lds0 + L_ONES + (dq == 5 ? 0 : 8);
        const unsigned vstep = real ? 64u : 0u;
        f32x16 o = zero;
#pragma unroll
        for (int j = 0; j < 9; ++j) {
#pragma unroll
            for (int r = 0; r < 16; ++r) s[j][r] = __builtin_amdgcn_exp2f(s[j][r] - m);
#pragma unroll
            for (int ss = 0; ss < 2; ++ss) {
                u32x4 pw;
#pragma unroll
                for (int i = 0; i < 4; ++i) pw[i] = pk2(s[j][8 * ss + 2 * i], s[j][8 * ss + 2 * i + 1]);
                o = MFMA32(cat8(vtr(vbase + vstep * (4 * j + 2 * ss)), vtr(vbase + vstep * (4 * j + 2 * ss + 1))), __builtin_bit_cast(bf16x8, pw), o);
            }
        }
        float l; { auto rr = __builtin_amdgcn_permlane32_swap(__float_as_uint(o[8]), __float_as_uint(o[8]), false, false); l = h ? o[8] : __uint_as_float(rr[1]); }
        const float inv = __builtin_amdgcn_rcpf(l);
        const int tok = 256 * qb + 32 * wid + cl;
        const int a5 = (head >> 1) * 5, odd = head & 1;
        const unsigned act = WS_ACT + (unsigned)((tok >> 4) * NKS3 + 54) * 1024u + (tok & 15) * 16;
        const int slot = a5 + odd * 2 + h;
        st128(rws, act + (slot >> 2) * 1024 + (slot & 3) * 256, (u32x4){pk2(o[0] * inv, o[1] * inv), pk2(o[2] * inv, o[3] * inv), pk2(o[4] * inv, o[5] * inv), pk2(o[6] * inv, o[7] * inv)});
        if (h == 0) {
            const int s4 = a5 + 4;
            st64(rws, act + (s4 >> 2) * 1024 + (s4 & 3) * 256 + odd * 8, (u32x2){pk2(o[8] * inv, o[9] * inv), pk2(o[10] * inv, o[11] * inv)});
        }
    }
    ST(3);
    const unsigned fo = FL_O + ((qb * 8 + wid) * 32 + head) * 4;
    if (wid >= 4) {
        WAIT_LGKM0();
        BAR();
        set_flag(ws, fo, tag, lane);
        return;
    }
    {
        const int tok = mt * 64 + wid * 16 + (lane & 15), rq = lane >> 4;
        const unsigned off = (unsigned)(tok * D_ + nt * 80 + rq * 20) * 2u;
        u32x2 xr[5];
#pragma unroll
        for (int ng = 0; ng < 5; ++ng) xr[ng] = *(const u32x2*)(x + off + ng * 8);
        f32x4 acc[5];
#pragma unroll
        for (int i = 0; i < 5; ++i) acc[i] = (f32x4){0.f, 0.f, 0.f, 0.f};
        bf16x8 aa[2], wa[2][5], ab[2], wb[2][5];
        const unsigned wbase = lds0 + lane * 16, abase = lds0 + 20480 + wid * 4096 + lane * 16;
#define RDA(dst, k) dst = ldsr128(as + (k) * 1024)
#define RDW(dst, k, ng) dst = ldsr128(wsb + (k) * 5120 + (ng) * 1024)
#define PHASE(af, wf, na, nw, hf) do { \
        _Pragma("unroll") for (int k = 0; k < 2; ++k) { \
            acc[0] = MFMA16(wf[k][0], af[k], acc[0]); SBAR(); RDA(na[k], (hf) * 2 + k); SBAR(); \
            acc[1] = MFMA16(wf[k][1], af[k], acc[1]); SBAR(); RDW(nw[k][0], (hf) * 2 + k, 0); SBAR(); \
            acc[2] = MFMA16(wf[k][2], af[k], acc[2]); SBAR(); RDW(nw[k][1], (hf) * 2 + k, 1); RDW(nw[k][2], (hf) * 2 + k, 2); SBAR(); \
            acc[3] = MFMA16(wf[k][3], af[k], acc[3]); SBAR(); RDW(nw[k][3], (hf) * 2 + k, 3); SBAR(); \
            acc[4] = MFMA16(wf[k][4], af[k], acc[4]); SBAR(); RDW(nw[k][4], (hf) * 2 + k, 4); SBAR(); \
        } } while (0)
        WAIT_LGKM0();
        BAR();
        ST(4);
        { const unsigned as = abase, wsb = wbase;
#pragma unroll
          for (int k = 0; k < 2; ++k) { RDA(aa[k], k);
#pragma unroll
            for (int ng = 0; ng < 5; ++ng) RDW(wa[k][ng], k, ng); } }
        for (int st = 0; st < P3_NST; ++st) {
            { const unsigned so = (st & 3) * P3_SLOT; const unsigned as = abase + so, wsb = wbase + so; PHASE(aa, wa, ab, wb, 1); }
            if (st == 1) set_flag(ws, fo, tag, lane);
            WAIT_LGKM0();
            BAR();
            if (st == 4) ST(5);
            if (st == 12) ST(6);
            { const unsigned so = ((st + 1) & 3) * P3_SLOT; const unsigned as = abase + so, wsb = wbase + so; PHASE(ab, wb, aa, wa, 0); }
        }
#undef PHASE
#undef RDA
#undef RDW
        ST(7);
#pragma unroll
        for (int ng = 0; ng < 5; ++ng) {
            const float r0 = __uint_as_float(xr[ng].x << 16) + acc[ng][0], r1 = __uint_as_float(xr[ng].x & 0xffff0000u) + acc[ng][1];
            const float r2 = __uint_as_float(xr[ng].y << 16) + acc[ng][2], r3 = __uint_as_float(xr[ng].y & 0xffff0000u) + acc[ng][3];
            *(u32x2*)(out + off + ng * 8) = (u32x2){pk2(r0, r1), pk2(r2, r3)};
        }
        ST(8);
    }
    if (wid == 0) ST_OUT(0);
}

extern "C" void kernel_launch(void* const* d_in, const int* in_sizes, int n_in, void* d_out, int out_size, void* d_ws, size_t ws_size, hipStream_t stream) {
    (void)hipFuncSetAttribute((const void*)mega_k, hipFuncAttributeMaxDynamicSharedMemorySize, LDS_BYTES);
    mega_k<<<256, 768, LDS_BYTES, stream>>>((const char*)d_in[0], (const char*)d_in[1], (const char*)d_in[2], (const char*)d_in[4], (const char*)d_in[5],
                                            (const char*)d_in[6], (char*)d_ws, (char*)d_out);
}
```

```cpp
#include <hip/hip_runtime.h>
#include <hip/hip_bf16.h>
#include <stdint.h>

typedef unsigned short u16;
typedef short bf16x8 __attribute__((ext_vector_type(8)));
typedef short s16x4 __attribute__((ext_vector_type(4)));
typedef float f32x16 __attribute__((ext_vector_type(16)));
typedef float f32x4 __attribute__((ext_vector_type(4)));
typedef float f32x2 __attribute__((ext_vector_type(2)));
typedef unsigned u32x4 __attribute__((ext_vector_type(4)));
typedef unsigned u32x2 __attribute__((ext_vector_type(2)));
typedef __bf16 bf16x2_t __attribute__((ext_vector_type(2)));
typedef int srd_t __attribute__((ext_vector_type(4)));
typedef short v4i16_t __attribute__((ext_vector_type(4)));
#define LAS __attribute__((address_space(3)))
#define GAS __attribute__((address_space(1)))
#define DI __device__ __forceinline__

constexpr int D_ = 640, DH = 20;
constexpr int NKS3 = 74;
constexpr unsigned FL_E = 0, FL_TMO = 128, FL_S = 1024, FL_Q = 2048, FL_H = 3072, FL_O = 4096, FL_W = 5120, FL_ST = 6144, FL_CQ = 7168, FL_CG = 7424;
constexpr unsigned WS_STAT = 16384;
constexpr unsigned WS_CSQ = 32768;
constexpr unsigned WS_CSG = WS_CSQ + 8192;
constexpr unsigned WS_QKV = 65536;
constexpr unsigned WS_ACT = WS_QKV + 480u * 16384u;
constexpr unsigned WS_WT3 = WS_ACT + 128u * NKS3 * 1024u;
constexpr unsigned WS_END = WS_WT3 + 8u * NKS3 * 5u * 1024u;
constexpr int L_QS = 131072, L_QC = L_QS + 1024;
constexpr int AT_PL = 4096 + 32;
constexpr int L_K = 110592, L_V = L_K + 5 * AT_PL, L_ONES = L_K + 10 * AT_PL, L_QP = L_ONES + 64, L_TAG = L_QP + 10240, L_CNT = L_TAG + 16, L_GO = L_TAG + 48,
              L_CSG = L_TAG + 64, LDS_BYTES = 163840;
static_assert(L_CSG + 1024 <= LDS_BYTES && (L_CSG & 15) == 0, "lds");
constexpr int P3_SLOT = 36864, P3_NST = 19;

DI unsigned pk2(float a, float b) { f32x2 v = {a, b}; bf16x2_t r = __builtin_convertvector(v, bf16x2_t); return __builtin_bit_cast(unsigned, r); }
DI s16x4 vtr(unsigned a) { return __builtin_bit_cast(s16x4, __builtin_amdgcn_ds_read_tr16_b64_v4i16((LAS v4i16_t*)(size_t)a)); }
DI bf16x8 cat8(s16x4 a, s16x4 b) { return __builtin_shufflevector(a, b, 0, 1, 2, 3, 4, 5, 6, 7); }
DI bf16x8 ldsr128(unsigned a) { return *(const LAS bf16x8*)(size_t)a; }
DI float bflo(unsigned v) { return __uint_as_float(v << 16); }
DI float bfhi(unsigned v) { return __uint_as_float(v & 0xffff0000u); }
DI srd_t make_srd(const void* base, unsigned bytes) {
    const uint64_t v = (uint64_t)base;
    srd_t r; r[0] = (int)(unsigned)v; r[1] = (int)((unsigned)(v >> 32) & 0xffffu); r[2] = (int)bytes; r[3] = 0x00020000; return r;
}
template <int OFF, int SC> DI void bdma(unsigned voff, srd_t srd, unsigned soff, unsigned dst) {
    if (SC) asm volatile("s_mov_b32 m0, %3\n\ts_nop 0\n\tbuffer_load_dwordx4 %0, %1, %2 offen offset:%4 sc1 lds" ::"v"(voff), "s"(srd), "s"(soff), "s"(dst), "n"(OFF) : "memory");
    else asm volatile("s_mov_b32 m0, %3\n\ts_nop 0\n\tbuffer_load_dwordx4 %0, %1, %2 offen offset:%4 lds" ::"v"(voff), "s"(srd), "s"(soff), "s"(dst), "n"(OFF) : "memory");
}
template <int OFF, int SC> DI void bdma_m0(unsigned voff, srd_t srd, unsigned soff) {
    if (SC) asm volatile("buffer_load_dwordx4 %0, %1, %2 offen offset:%3 sc1 lds" ::"v"(voff), "s"(srd), "s"(soff), "n"(OFF) : "memory");
    else asm volatile("buffer_load_dwordx4 %0, %1, %2 offen offset:%3 lds" ::"v"(voff), "s"(srd), "s"(soff), "n"(OFF) : "memory");
}
#define WAIT_VM(n) asm volatile("s_waitcnt vmcnt(%0)" ::"n"(n) : "memory")
#define WAIT_LGKM0() asm volatile("s_waitcnt lgkmcnt(0)" ::: "memory")
#define BAR() do { asm volatile("" ::: "memory"); __builtin_amdgcn_s_barrier(); asm volatile("" ::: "memory"); } while (0)
#define SBAR() __builtin_amdgcn_sched_barrier(0)
#define PIN(x) asm volatile("" : "+v"(x))
#define MFMA32(a, b, c) __builtin_amdgcn_mfma_f32_32x32x16_bf16((a), (b), (c), 0, 0, 0)
#define MFMA16(a, b, c) __builtin_amdgcn_mfma_f32_16x16x32_bf16((a), (b), (c), 0, 0, 0)
#define RLX_AGENT __ATOMIC_RELAXED, __HIP_MEMORY_SCOPE_AGENT
typedef GAS unsigned gu32;

#define ST(i)
#define ST_DECL
#define ST_OUT(kid)

DI void st64(__amdgpu_buffer_rsrc_t rs, unsigned off, u32x2 v) { __builtin_amdgcn_raw_buffer_store_b64(v, rs, off, 0, 16); }
DI void st128(__amdgpu_buffer_rsrc_t rs, unsigned off, u32x4 v) { __builtin_amdgcn_raw_buffer_store_b128(v, rs, off, 0, 16); }
DI void signal_wg(char* ws, unsigned cnt, unsigned nwaves, unsigned off, int nf, unsigned tag, int lane) {
    WAIT_VM(0);
    unsigned old = 0;
    if (lane == 0) old = __hip_atomic_fetch_add((LAS unsigned*)(size_t)cnt, 1u, __ATOMIC_RELAXED, __HIP_MEMORY_SCOPE_WORKGROUP);
    old = __builtin_amdgcn_readfirstlane(old);
    if (old == nwaves - 1 && lane < nf) __hip_atomic_store((gu32*)(ws + off + lane * 64), tag, RLX_AGENT);
}
DI unsigned flag_load(const char* ws, unsigned off) { unsigned v; asm volatile("global_load_dword %0, %1, %2 sc1" : "=v"(v) : "v"(off), "s"(ws) : "memory"); return v; }
DI void poll1(char* ws, unsigned off, unsigned tag) {
    for (unsigned spins = 0;; ++spins) {
        unsigned v = flag_load(ws, off);
        asm volatile("s_waitcnt vmcnt(0)" : "+v"(v)::"memory");
        if (__all(v == tag)) break;
        if (spins > (1u << 17)) { __hip_atomic_store((gu32*)(ws + FL_TMO), 1u, RLX_AGENT); break; }
        __builtin_amdgcn_s_sleep(1);
    }
}

__global__ __launch_bounds__(768, 3) void mega_k(const char* __restrict__ x, const char* __restrict__ wqkv, const char* __restrict__ wo, const char* __restrict__ wg,
                                                 const char* __restrict__ wu, const char* __restrict__ wd, char* __restrict__ ws, char* __restrict__ out) {
    extern __shared__ __attribute__((aligned(16))) char lds[];
    ST_DECL ST(0);
    const int tid = threadIdx.x, lane = tid & 63, wid = __builtin_amdgcn_readfirstlane(tid >> 6);
    const int b = blockIdx.x, xcd = b & 7, idx = b >> 3;
    const int panel = (xcd & 1) * 8 + (idx & 7), c = (xcd >> 1) * 4 + (idx >> 3);
    const int qb = xcd, head = idx;
    const int mt = xcd * 4 + (idx & 3), nt = idx >> 2;
    const unsigned lds0 = (unsigned)(size_t)lds;
    const bool helper = c == 15;
    const int ct = helper ? panel : c;
    const bool hasQ = c < 15, hasG = ct < 14 && c != 14;
    const int nret = c == 15 ? 3 : (c == 14 ? (64 + panel < NKS3 ? 2 : 1) : 0);
    const int ret0 = c == 15 ? panel : 48 + panel;

    if (wid >= 8) {
        const int l = wid - 8;
        const int r8 = lane >> 3, cc = lane & 7;
        const unsigned csw = (cc ^ (((r8 >> 1) & 1) << 2)) * 16;
        const unsigned vq = r8 * 3840 + csw, vg = r8 * 3456 + csw;
        const unsigned vx0 = r8 * 1280 + ((cc ^ (r8 >> 1)) * 16), vx1 = r8 * 1280 + ((cc ^ (4 + (r8 >> 1))) * 16);
        const unsigned vl = lane * 16;
        const srd_t sx = make_srd(x + (unsigned)(panel * 128 * 1280), helper ? 0 : 128 * 1280);
        auto issue_x = [&](int kb, unsigned dst) {
            if (!helper) {
#pragma unroll
                for (int i = 0; i < 4; ++i) bdma<0, 0>((i & 1) ? vx1 : vx0, sx, (4 * l + i) * 10240 + kb * 128, dst + (4 * l + i) * 1024);
            }
        };
        auto issue_ret = [&](int u, unsigned dst) {
            const srd_t sr = u < 54 ? make_srd(wd, 1728u * 1280u) : make_srd(wo, 640u * 1280u);
#pragma unroll 1
            for (int i = 0; i < 10; ++i) {
                const int ci = (l * 10 + i) * 64 + lane, sr_ = ci / 80, ch = ci - sr_ * 80;
                const int kg = sr_ >> 3, j = sr_ & 7;
                int row;
                if (u < 54) row = 32 * u + 16 * (kg >> 1) + 8 * (j >> 2) + 4 * (kg & 1) + (j & 3);
                else {
                    const int slot = 4 * (u - 54) + kg, a = slot / 5, ii = slot - 5 * a;
                    row = ii < 4 ? (2 * a + (ii >> 1)) * DH + (ii & 1) * 4 + (j & 3) + 8 * (j >> 2) : (2 * a + (j >> 2)) * DH + 16 + (j & 3);
                }
                bdma<0, 0>(row * 1280 + ch * 16, sr, 0, dst + (l * 10 + i) * 1024);
            }
        };
        const int np = ct < 13 ? 2 : 1;
        const srd_t s0 = make_srd(wg + ct * 256, 640u * 3456u - ct * 256), s1 = make_srd(wu + ct * 256, 640u * 3456u - ct * 256);
        const srd_t s2 = make_srd(wg + ct * 256 + 128, np == 2 ? 640u * 3456u - ct * 256 - 128 : 0u), s3 = make_srd(wu + ct * 256 + 128, np == 2 ? 640u * 3456u - ct * 256 - 128 : 0u);
        auto issue_w = [&](int st, int msk) {
            const unsigned dw = lds0 + (st & msk) * 16384 + l * 1024, so = (st * 32 + l * 8) * 3456;
            bdma<0, 0>(vg, s0, so, dw); bdma<0, 0>(vg, s1, so, dw + 4096); bdma<0, 0>(vg, s2, so, dw + 8192); bdma<0, 0>(vg, s3, so, dw + 12288);
        };
        const srd_t sw = make_srd(wqkv + ct * 256, 640u * 3840u - ct * 256);
        auto issue_qw = [&](int st, unsigned dst) {
#pragma unroll
            for (int i = 0; i < 4; ++i) { const int j = 4 * l + i; bdma<0, 0>(vq, sw, (st * 64 + 8 * (j & 7)) * 3840 + (j >> 3) * 128, dst + j * 1024); }
        };
        if (!helper) {
            auto issue = [&](int st) { const unsigned sl = lds0 + (st & 3) * 32768; issue_qw(st, sl + 16384); issue_x(st, sl); };
            issue(0);
            __builtin_amdgcn_s_sleep(4);
            issue(1); issue(2);
#pragma unroll 1
            for (int st = 0; st <= 6; ++st) { WAIT_VM(16); BAR(); issue(st + 3); }
            WAIT_VM(16); BAR();
            if (hasG) { issue_x(0, lds0 + 65536); WAIT_VM(12); } else { WAIT_VM(8); }
            BAR();
            if (hasG) { WAIT_VM(4); } else { WAIT_VM(0); }
            BAR();
            if (hasG) { issue_w(0, 3); issue_w(1, 3); }
            BAR();
            BAR();
            if (hasG) {
                issue_x(1, lds0 + 65536 + 16384); issue_w(2, 3);
#pragma unroll 1
                for (int st = 0; st <= 20; ++st) {
                    if (st >= 18) { WAIT_VM(0); } else if (st >= 2 && !(st & 1)) { WAIT_VM(8); } else { WAIT_VM(12); }
                    BAR();
                    if (!(st & 1) && st >= 2 && (st >> 1) + 1 < 10) issue_x((st >> 1) + 1, lds0 + 65536 + (((st >> 1) + 1) & 1) * 16384);
                    if (st + 3 < 20) issue_w(st + 3, 3);
                }
            }
        } else {
            if (hasG) {
#pragma unroll 1
                for (int st = 0; st < 7; ++st) issue_w(st, 7);
#pragma unroll 1
                for (int st = 0; st <= 20; ++st) {
                    if (st <= 13) { WAIT_VM(24); } else { WAIT_VM(0); }
                    BAR();
                    if (st + 7 < 20) issue_w(st + 7, 7);
                }
            }
        }
        if (nret) {
            issue_ret(ret0, lds0);
            if (nret > 1) issue_ret(ret0 + 16, lds0 + 40960);
            if (nret > 2) issue_ret(ret0 + 32, lds0 + 81920);
            WAIT_VM(0);
            BAR();
            BAR();
        }
        BAR();
        ST(1);
        const unsigned tag = __builtin_amdgcn_readfirstlane(*(const volatile LAS unsigned*)(size_t)(lds0 + L_TAG));
        if (b == 0 && l == 1) {
#pragma unroll 1
            for (int i = 0; i < 4; ++i) poll1(ws, FL_S + (i * 64 + lane) * 4, tag);
            if (lane == 0) __hip_atomic_store((gu32*)(ws + FL_E), tag, RLX_AGENT);
        }
        {
            if (l == 0) {
                poll1(ws, FL_W + lane * 4, tag); poll1(ws, FL_W + (64 + (lane < 10 ? lane : 0)) * 4, tag);
                ST(4);
                poll1(ws, FL_H + ((mt >> 1) * 16 + (lane < 14 ? lane : 0)) * 4, tag);
                if (lane == 0) *(volatile LAS unsigned*)(size_t)(lds0 + L_GO) = tag;
            } else {
                for (unsigned spins = 0; *(const volatile LAS unsigned*)(size_t)(lds0 + L_GO) != tag && spins < (1u << 22); ++spins) __builtin_amdgcn_s_sleep(2);
            }
            ST(2);
            const srd_t sa = make_srd(ws + WS_ACT + (unsigned)((mt * 4 + l) * NKS3 * 1024), NKS3 * 1024);
            const srd_t sw = make_srd(ws + WS_WT3 + (unsigned)(nt * NKS3 * 5 * 1024), NKS3 * 5 * 1024);
            auto issue = [&](int st) {
                const unsigned sl = lds0 + (st & 3) * P3_SLOT, ow = st * 20480 + l * 5120;
                bdma<0, 1>(vl, sw, ow, sl + l * 5120); bdma_m0<1024, 1>(vl, sw, ow); bdma_m0<2048, 1>(vl, sw, ow); bdma_m0<3072, 1>(vl, sw, ow);
                bdma<0, 1>(vl, sw, ow + 4096, sl + l * 5120 + 4096);
                bdma<0, 1>(vl, sa, st * 4096, sl + 20480 + l * 4096); bdma_m0<1024, 1>(vl, sa, st * 4096); bdma_m0<2048, 1>(vl, sa, st * 4096); bdma_m0<3072, 1>(vl, sa, st * 4096);
            };
            auto step = [&](int st) {
                if (st + 2 < P3_NST) { WAIT_VM(18); } else if (st + 1 < P3_NST) { WAIT_VM(9); } else { WAIT_VM(0); }
                BAR();
                if (st + 3 < P3_NST) issue(st + 3);
            };
            issue(0);
            __builtin_amdgcn_s_sleep(4);
            issue(1); issue(2);
            ST(5);
#pragma unroll 1
            for (int st = 0; st <= 6; ++st) step(st);
            const unsigned ofo = FL_O + (qb * 32 + (lane & 31)) * 4;
            unsigned pf = flag_load(ws, ofo);
#pragma unroll 1
            for (int st = 7; st <= 9; ++st) step(st);
            asm volatile("s_waitcnt vmcnt(27)" : "+v"(pf)::"memory");
            if (!__all(pf == tag)) poll1(ws, ofo, tag);
#pragma unroll 1
            for (int st = 10; st <= P3_NST; ++st) step(st);
        }
        ST(3);
        ST_OUT(4 + l);
        return;
    }

    const int cl = lane & 31, h = lane >> 5;
    const int g = lane >> 4, q = (lane & 15) >> 2, p = lane & 3;
    const int th = wid & 1;
    const __amdgpu_buffer_rsrc_t rws = __builtin_amdgcn_make_buffer_rsrc(ws, 0, WS_END, 0x00020000);
    const unsigned wlane = (8 * (g >> 1) + q) * 128 + (p & 1) * 8;
    const unsigned xrow = lds0 + (64 * th + cl) * 128, xsw = (cl >> 1) & 7;
    const bf16x8 ONES8 = {0x3f80, 0x3f80, 0x3f80, 0x3f80, 0x3f80, 0x3f80, 0x3f80, 0x3f80};
    float mean[2] = {0.f, 0.f}, rstd[2] = {0.f, 0.f};
    unsigned tag = 0;
    if (wid == 0) {
        if (lane < 2) *(LAS u32x2*)(size_t)(lds0 + L_ONES + lane * 8) = (u32x2){0x00003f80u, 0u};
        if (lane >= 4 && lane < 13) *(LAS unsigned*)(size_t)(lds0 + L_CNT + (lane - 4) * 4) = 0u;
        WAIT_LGKM0();
    } else if (wid == 1) {
        const unsigned e = __hip_atomic_load((gu32*)(ws + FL_E), RLX_AGENT);
        const int tok = panel * 128 + 8 * c + (lane >> 3);
        u32x4 v[10];
#pragma unroll
        for (int i = 0; i < 10; ++i) v[i] = *(const u32x4*)(x + (unsigned)(tok * 1280 + ((lane & 7) + 8 * i) * 16));
        tag = __builtin_amdgcn_readfirstlane(e) + 1u;
        if (lane == 0) *(LAS unsigned*)(size_t)(lds0 + L_TAG) = tag;
        float s = 0.f;
#pragma unroll
        for (int i = 0; i < 10; ++i)
#pragma unroll
            for (int j = 0; j < 4; ++j) s += bflo(v[i][j]) + bfhi(v[i][j]);
        s += __shfl_xor(s, 1); s += __shfl_xor(s, 2); s += __shfl_xor(s, 4);
        const float mu = s * (1.f / D_);
        float qq = 0.f;
#pragma unroll
        for (int i = 0; i < 10; ++i)
#pragma unroll
            for (int j = 0; j < 4; ++j) { const float d0 = bflo(v[i][j]) - mu, d1 = bfhi(v[i][j]) - mu; qq = __builtin_fmaf(d0, d0, qq); qq = __builtin_fmaf(d1, d1, qq); }
        qq += __shfl_xor(qq, 1); qq += __shfl_xor(qq, 2); qq += __shfl_xor(qq, 4);
        const float rs = 1.f / __builtin_sqrtf(qq * (1.f / D_) + 1e-5f);
        if ((lane & 7) == 0) st64(rws, WS_STAT + tok * 8, (u32x2){__float_as_uint(mu), __float_as_uint(rs)});
        WAIT_LGKM0();
    }
    auto stat_flag = [&]() {
        if (wid == 1) {
            WAIT_VM(0);
            if (lane == 0) { __hip_atomic_store((gu32*)(ws + FL_ST) + panel * 16 + c, tag, RLX_AGENT); __hip_atomic_store((gu32*)(ws + FL_S) + b, tag, RLX_AGENT); }
        }
    };
    unsigned att_fo;
    {
        const int part = lane >> 3, cand = (lane >> 2) & 1, pn = lane & 3;
        const int f0 = part * 640 + head * DH + (cand ? DH - 1 : 0);
        int pp = 2 * qb - 2 + pn; if (pp < 0) pp = 0;
        att_fo = FL_Q + (pp * 16 + (lane < 24 ? (f0 >> 7) : 0)) * 4;
    }
    auto issue_att = [&]() {
        const srd_t sq = make_srd(ws + WS_QKV, 480u * 16384u);
        const int kb0 = 256 * qb - 256;
        const unsigned vl = lane * 16;
#pragma unroll
        for (int i = 0; i < 5; ++i) {
            const int pc = wid * 5 + i, pl = pc >> 2, qu = pc & 3;
            int key0 = kb0 + qu * 128; if (key0 < 0) key0 += 256;
            const int gp = (pl < 5 ? 160 : 315) + head * 5 + pl;
            bdma<0, 1>(vl, sq, gp * 16384 + key0 * 8, lds0 + L_K + pl * AT_PL + qu * 1024);
        }
        bdma<0, 1>(vl, sq, (head * 5 + (wid >> 1)) * 16384 + (256 * qb + (wid & 1) * 128) * 8, lds0 + L_QP + wid * 1024);
        if (wid < 2) bdma<0, 1>(vl, sq, (head * 5 + 4) * 16384 + (256 * qb + wid * 128) * 8, lds0 + L_QP + (8 + wid) * 1024);
    };

    if (hasQ) {
        const int blk = wid >> 1, unit = blk >> 1, fh = blk & 1;
        const unsigned wad = lds0 + 16384 + unit * 8192 + wlane + (((fh * 4 + (g & 1) * 2 + (p >> 1)) ^ (((q >> 1) & 1) << 2)) * 16);
        unsigned xad[4];
#pragma unroll
        for (int ks = 0; ks < 4; ++ks) xad[ks] = xrow + (((2 * ks + h) ^ xsw) * 16);
        f32x16 acc[2], accs;
#pragma unroll
        for (int r = 0; r < 16; ++r) { acc[0][r] = 0.f; acc[1][r] = 0.f; accs[r] = 0.f; }
        BAR();
        ST(9);
        tag = __builtin_amdgcn_readfirstlane(*(const LAS unsigned*)(size_t)(lds0 + L_TAG));
        bf16x8 xa[2][2], wa[2], xb[2][2], wb[2];
#define Q_RDK(xx, ww, so, k0, k) do { xx[k][0] = ldsr128(xad[(k0) + k] + (so)); xx[k][1] = ldsr128(xad[(k0) + k] + (so) + 4096); \
            ww[k] = cat8(vtr(wad + (so) + ((k0) + k) * 2048), vtr(wad + (so) + ((k0) + k) * 2048 + 512)); } while (0)
#define Q_PHASE(xc, wc, xn, wn, so, k0, sel) do { \
            acc[0] = MFMA32(wc[0], xc[0][0], acc[0]); SBAR(); Q_RDK(xn, wn, so, k0, 0); SBAR(); \
            acc[1] = MFMA32(wc[0], xc[0][1], acc[1]); \
            acc[0] = MFMA32(wc[1], xc[1][0], acc[0]); SBAR(); Q_RDK(xn, wn, so, k0, 1); SBAR(); \
            acc[1] = MFMA32(wc[1], xc[1][1], acc[1]); \
            if (th == (sel)) { accs = MFMA32(wc[0], ONES8, accs); accs = MFMA32(wc[1], ONES8, accs); } \
            SBAR(); } while (0)
#define Q_STEP(st) do { const unsigned so = ((st) & 3) * 32768, sn = (((st) + 1) & 3) * 32768; \
            Q_PHASE(xa, wa, xb, wb, so, 2, 0); WAIT_LGKM0(); BAR(); Q_PHASE(xb, wb, xa, wa, sn, 0, 1); } while (0)
        Q_RDK(xa, wa, 0, 0, 0); Q_RDK(xa, wa, 0, 0, 1);
        Q_STEP(0);
        stat_flag();
#pragma unroll 1
        for (int st = 1; st < 7; ++st) Q_STEP(st);
        const unsigned efo = FL_ST + (panel * 16 + (lane & 15)) * 4;
        unsigned pf = flag_load(ws, efo);
        Q_STEP(7); Q_STEP(8);
        asm volatile("s_waitcnt vmcnt(0)" : "+v"(pf)::"memory");
        if (!__all(pf == tag)) poll1(ws, efo, tag);
        u32x2 sv0, sv1;
        {
            const unsigned so_ = WS_STAT + (panel * 128 + 64 * th + cl) * 8;
            asm volatile("global_load_dwordx2 %0, %2, %3 sc1\n\tglobal_load_dwordx2 %1, %2, %3 offset:256 sc1" : "=&v"(sv0), "=&v"(sv1) : "v"(so_), "s"(ws) : "memory");
        }
        Q_STEP(9);
#undef Q_STEP
#undef Q_PHASE
#undef Q_RDK
        ST(10);
        if (cl == 0) {
#pragma unroll
            for (int gg = 0; gg < 4; ++gg) *(LAS f32x4*)(size_t)(lds0 + L_QC + (th * 128 + blk * 32 + 8 * gg + 4 * h) * 4) = (f32x4){accs[4 * gg], accs[4 * gg + 1], accs[4 * gg + 2], accs[4 * gg + 3]};
        }
        WAIT_LGKM0();
        BAR();
        asm volatile("s_waitcnt vmcnt(0)" : "+v"(sv0), "+v"(sv1)::"memory");
        mean[0] = __uint_as_float(sv0.x); rstd[0] = __uint_as_float(sv0.y); mean[1] = __uint_as_float(sv1.x); rstd[1] = __uint_as_float(sv1.y);
        const int f40 = (2 * c + unit) * 16 + fh * 8 + h;
        const float sc = (2 * c + unit < 10) ? 0.22360679774997896f * 1.4426950408889634f : 1.f;
        f32x4 cs[4];
#pragma unroll
        for (int gg = 0; gg < 4; ++gg) { const unsigned ca = lds0 + L_QC + (blk * 32 + 8 * gg + 4 * h) * 4; cs[gg] = *(const LAS f32x4*)(size_t)ca + *(const LAS f32x4*)(size_t)(ca + 512); }
#pragma unroll
        for (int t = 0; t < 2; ++t) {
            const int tok = panel * 128 + th * 64 + t * 32 + cl;
            const float rs = rstd[t] * sc, nm = -mean[t];
#pragma unroll
            for (int gg = 0; gg < 4; ++gg) {
                float v[4];
#pragma unroll
                for (int j = 0; j < 4; ++j) v[j] = __builtin_fmaf(nm, cs[gg][j], acc[t][4 * gg + j]) * rs;
                st64(rws, WS_QKV + (unsigned)(f40 + 2 * gg) * 16384u + tok * 8, (u32x2){pk2(v[0], v[1]), pk2(v[2], v[3])});
            }
        }
        if (!hasG) signal_wg(ws, lds0 + L_CNT, 8, FL_Q + (panel * 16 + c) * 4, 1, tag, lane);
    }
    ST(1);
    if (hasG) {
        const int fh = (wid >> 1) & 1, pr = wid >> 2;
        const bool act = (ct < 13) || pr == 0;
        const unsigned wad = lds0 + pr * 8192 + wlane + (((fh * 4 + (g & 1) * 2 + (p >> 1)) ^ (((q >> 1) & 1) << 2)) * 16);
        BAR();
        ST(11);
        if (!helper) {
            unsigned xad[4];
#pragma unroll
            for (int ks = 0; ks < 4; ++ks) xad[ks] = xrow + 65536 + (((2 * ks + h) ^ xsw) * 16);
            f32x16 acc[2][2];
#pragma unroll
            for (int r = 0; r < 16; ++r) { acc[0][0][r] = 0.f; acc[0][1][r] = 0.f; acc[1][0][r] = 0.f; acc[1][1][r] = 0.f; }
            bf16x8 xa[2], wa[2], xb[2], wb[2];
#define G_PHASE(xf, wf, nx, nw, so_w, ks, so_x, kx) do { \
            acc[0][0] = MFMA32(wf[0], xf[0], acc[0][0]); SBAR(); nx[0] = ldsr128(xad[kx] + (so_x)); nx[1] = ldsr128(xad[kx] + (so_x) + 4096); SBAR(); \
            acc[1][0] = MFMA32(wf[0], xf[1], acc[1][0]); SBAR(); nw[0] = cat8(vtr(wad + (so_w) + (ks) * 2048), vtr(wad + (so_w) + (ks) * 2048 + 512)); SBAR(); \
            acc[0][1] = MFMA32(wf[1], xf[0], acc[0][1]); SBAR(); nw[1] = cat8(vtr(wad + (so_w) + 4096 + (ks) * 2048), vtr(wad + (so_w) + 4096 + (ks) * 2048 + 512)); SBAR(); \
            acc[1][1] = MFMA32(wf[1], xf[1], acc[1][1]); SBAR(); } while (0)
#define G_STEP2(s2) do { \
            const unsigned sx0 = ((s2) & 1) * 16384, sx1 = (((s2) + 1) & 1) * 16384; \
            const unsigned w0 = ((2 * (s2)) & 3) * 16384, w1 = ((2 * (s2) + 1) & 3) * 16384, w2 = ((2 * (s2) + 2) & 3) * 16384; \
            if (act) G_PHASE(xa, wa, xb, wb, w0, 1, sx0, 1); \
            WAIT_LGKM0(); BAR(); \
            if (act) { G_PHASE(xb, wb, xa, wa, w1, 0, sx0, 2); G_PHASE(xa, wa, xb, wb, w1, 1, sx0, 3); } \
            WAIT_LGKM0(); BAR(); \
            if (act) G_PHASE(xb, wb, xa, wa, w2, 0, sx1, 0); } while (0)
            xa[0] = ldsr128(xad[0]); xa[1] = ldsr128(xad[0] + 4096);
            wa[0] = cat8(vtr(wad), vtr(wad + 512)); wa[1] = cat8(vtr(wad + 4096), vtr(wad + 4096 + 512));
#pragma unroll 1
            for (int s2 = 0; s2 < 2; ++s2) G_STEP2(s2);
            signal_wg(ws, lds0 + L_CNT, 8, FL_Q + (panel * 16 + c) * 4, 1, tag, lane);
#pragma unroll 1
            for (int s2 = 2; s2 < 4; ++s2) G_STEP2(s2);
            const unsigned gfo = lane < 24 ? att_fo : FL_CG + c * 4;
            unsigned pf = flag_load(ws, gfo);
#pragma unroll 1
            for (int s2 = 4; s2 < 6; ++s2) G_STEP2(s2);
            asm volatile("s_waitcnt vmcnt(0)" : "+v"(pf)::"memory");
            const bool att_ok = __all(pf == tag);
            auto issue_more = [&]() { issue_att(); if (wid == 2) bdma<0, 1>(lane * 16, make_srd(ws, WS_QKV), WS_CSG + c * 1024, lds0 + L_CSG); };
            if (att_ok) issue_more();
#pragma unroll 1
            for (int s2 = 6; s2 < 10; ++s2) G_STEP2(s2);
            ST(12);
#undef G_STEP2
#undef G_PHASE
            if (!att_ok) { poll1(ws, gfo, tag); issue_more(); }
            WAIT_VM(0);
            BAR();
            if (act) {
                const int ks3 = (2 * c + pr) * 2 + fh;
                const unsigned csa = lds0 + L_CSG + (pr * 128 + fh * 32) * 4;
#pragma unroll
                for (int t = 0; t < 2; ++t) {
                    const int tok = panel * 128 + th * 64 + t * 32 + cl;
                    const float rs2 = rstd[t] * rstd[t], nm = -mean[t];
#pragma unroll
                    for (int gp = 0; gp < 2; ++gp) {
                        float hv[8];
#pragma unroll
                        for (int gq = 0; gq < 2; ++gq) {
                            const int gg = 2 * gp + gq;
                            const unsigned ca = csa + (8 * gg + 4 * h) * 4;
                            const f32x4 cg = *(const LAS f32x4*)(size_t)(ca), cu = *(const LAS f32x4*)(size_t)(ca + 256);
#pragma unroll
                            for (int j = 0; j < 4; ++j)
                                hv[4 * gq + j] = __builtin_fmaf(nm, cg[j], acc[t][0][4 * gg + j]) * __builtin_fmaf(nm, cu[j], acc[t][1][4 * gg + j]) * rs2;
                        }
                        st128(rws, WS_ACT + (unsigned)((tok >> 4) * NKS3 + ks3) * 1024u + ((tok & 15) + 16 * (2 * gp + h)) * 16,
                              (u32x4){pk2(hv[0], hv[1]), pk2(hv[2], hv[3]), pk2(hv[4], hv[5]), pk2(hv[6], hv[7])});
                    }
                }
            }
        } else {
            tag = __builtin_amdgcn_readfirstlane(*(const LAS unsigned*)(size_t)(lds0 + L_TAG));
            stat_flag();
            f32x16 acc;
#pragma unroll
            for (int r = 0; r < 16; ++r) acc[r] = 0.f;
#pragma unroll 1
            for (int st = 0; st < 20; ++st) {
                const unsigned a = wad + (st & 7) * 16384 + th * 4096;
                const bf16x8 w0 = cat8(vtr(a), vtr(a + 512)), w1 = cat8(vtr(a + 2048), vtr(a + 2048 + 512));
                acc = MFMA32(w0, ONES8, acc); acc = MFMA32(w1, ONES8, acc);
                WAIT_LGKM0();
                BAR();
            }
            ST(12);
            if (cl == 0) {
#pragma unroll
                for (int gg = 0; gg < 4; ++gg)
                    st128(rws, WS_CSG + (ct * 256 + pr * 128 + th * 64 + fh * 32 + 8 * gg + 4 * h) * 4, __builtin_bit_cast(u32x4, (f32x4){acc[4 * gg], acc[4 * gg + 1], acc[4 * gg + 2], acc[4 * gg + 3]}));
            }
            signal_wg(ws, lds0 + L_CNT + 4, 8, FL_CG + ct * 4, 1, tag, lane);
        }
    }
    if (nret) {
        if (!hasQ && !hasG) stat_flag();
        ST(6);
        BAR();
        ST(7);
        tag = __builtin_amdgcn_readfirstlane(*(const LAS unsigned*)(size_t)(lds0 + L_TAG));
#pragma unroll 1
        for (int i = 0; i < nret; ++i) {
            const int u = ret0 + 16 * i;
            const unsigned ra = lds0 + i * 40960 + (8 * g + q) * 1280 + 160 * wid + 40 * p;
#pragma unroll
            for (int ng = 0; ng < 5; ++ng) {
                const bf16x8 f = cat8(vtr(ra + 8 * ng), vtr(ra + 8 * ng + 4 * 1280));
                st128(rws, WS_WT3 + (unsigned)((wid * NKS3 + u) * 5 + ng) * 1024u + lane * 16, __builtin_bit_cast(u32x4, f));
            }
        }
        WAIT_LGKM0();
        BAR();
        signal_wg(ws, lds0 + L_CNT + 8, 8, FL_W + ret0 * 4, nret, tag, lane);
        ST(8);
        poll1(ws, att_fo, tag);
        issue_att();
        WAIT_VM(0);
        BAR();
    }
    ST(2);
    {
        bf16x8 qf0, qf1;
        {
            const unsigned qp = lds0 + L_QP + (32 * wid + cl) * 8;
            const u32x2 a = *(const LAS u32x2*)(size_t)(qp + (2 * h) * 2048), bq = *(const LAS u32x2*)(size_t)(qp + (2 * h + 1) * 2048), e = *(const LAS u32x2*)(size_t)(qp + 4 * 2048);
            qf0 = __builtin_bit_cast(bf16x8, (u32x4){a.x, a.y, bq.x, bq.y});
            qf1 = __builtin_bit_cast(bf16x8, (u32x4){h ? 0u : e.x, h ? 0u : e.y, 0u, 0u});
        }
        const unsigned kp = lds0 + L_K + (2 * h) * AT_PL + (32 * wid + cl) * 8, kp4 = lds0 + L_K + 4 * AT_PL + (32 * wid + cl) * 8;
        const float NEG = -__builtin_inff();
        const int ch = cl - 4 * h;
        const int jmin = qb == 0 ? 8 - wid : 0;
        auto stile = [&](int j, const f32x16& cin) -> f32x16 {
            const u32x2 a = *(const LAS u32x2*)(size_t)(kp + j * 256), bq = *(const LAS u32x2*)(size_t)(kp + AT_PL + j * 256), e = *(const LAS u32x2*)(size_t)(kp4 + j * 256);
            const f32x16 t = MFMA32(__builtin_bit_cast(bf16x8, (u32x4){a.x, a.y, bq.x, bq.y}), qf0, cin);
            return MFMA32(__builtin_bit_cast(bf16x8, (u32x4){e.x, e.y, e.x, e.y}), qf1, t);
        };
        auto cfill = [&](float v, int kind) -> f32x16 {
            f32x16 r_;
#pragma unroll
            for (int r = 0; r < 16; ++r) { const int rr = (r & 3) + 8 * (r >> 2); r_[r] = kind == 0 ? v : ((rr > ch) == (kind == 1) ? v : NEG); }
            return r_;
        };
        auto tmax = [&](const f32x16& t, float m_) -> float {
#pragma unroll
            for (int r = 0; r < 16; r += 2) m_ = __builtin_fmaxf(__builtin_fmaxf(m_, t[r]), t[r + 1]);
            return m_;
        };
        float m;
        {
            f32x16 cur = stile(8, cfill(0.f, 2));
            const f32x16 z = cfill(0.f, 0);
            m = NEG;
            if (jmin == 0) { f32x16 nx = stile(0, cfill(0.f, 1)); m = tmax(cur, m); cur = nx; }
#pragma unroll 1
            for (int j = jmin > 1 ? jmin : 1; j < 8; ++j) { f32x16 nx = stile(j, z); m = tmax(cur, m); cur = nx; }
            m = tmax(cur, m);
        }
        { auto rr = __builtin_amdgcn_permlane32_swap(__float_as_uint(m), __float_as_uint(m), false, false);
          m = __builtin_fmaxf(__uint_as_float(rr[0]), __uint_as_float(rr[1])); }
        ST(13);
        if (hasG && !helper) signal_wg(ws, lds0 + L_CNT + 4, 8, FL_H + (panel * 16 + c) * 4, 1, tag, lane);
        ST(5);
        const int dq = 4 * (g & 1) + p;
        const bool real = dq < 5;
        const unsigned vbase = real ? lds0 + L_V + dq * AT_PL + (32 * wid + 4 * h + q) * 8 : lds0 + L_ONES + (dq == 5 ? 0 : 8);
        const unsigned vstep = real ? 64u : 0u;
        f32x16 o = cfill(0.f, 0);
        auto pv = [&](int j, f32x16 t) {
            const unsigned va = vbase + vstep * (4 * j);
            const bf16x8 v0 = cat8(vtr(va), vtr(va + vstep)), v1 = cat8(vtr(va + 2 * vstep), vtr(va + 3 * vstep));
#pragma unroll
            for (int r = 0; r < 16; ++r) t[r] = __builtin_amdgcn_exp2f(t[r]);
            u32x4 p0, p1;
#pragma unroll
            for (int i = 0; i < 4; ++i) { p0[i] = pk2(t[2 * i], t[2 * i + 1]); p1[i] = pk2(t[8 + 2 * i], t[8 + 2 * i + 1]); }
            o = MFMA32(v0, __builtin_bit_cast(bf16x8, p0), o);
            o = MFMA32(v1, __builtin_bit_cast(bf16x8, p1), o);
        };
        {
            int jc = 8;
            f32x16 cur = stile(8, cfill(-m, 2));
            const f32x16 nm = cfill(-m, 0);
            if (jmin == 0) { f32x16 nx = stile(0, cfill(-m, 1)); pv(8, cur); cur = nx; jc = 0; }
#pragma unroll 1
            for (int j = jmin > 1 ? jmin : 1; j < 8; ++j) { f32x16 nx = stile(j, nm); pv(jc, cur); cur = nx; jc = j; }
            pv(jc, cur);
        }
        float l; { auto rr = __builtin_amdgcn_permlane32_swap(__float_as_uint(o[8]), __float_as_uint(o[8]), false, false); l = h ? o[8] : __uint_as_float(rr[1]); }
        const float inv = __builtin_amdgcn_rcpf(l);
        const int tok = 256 * qb + 32 * wid + cl;
        const int a5 = (head >> 1) * 5, odd = head & 1;
        const unsigned act = WS_ACT + (unsigned)((tok >> 4) * NKS3 + 54) * 1024u + (tok & 15) * 16;
        const int slot = a5 + odd * 2 + h;
        st128(rws, act + (slot >> 2) * 1024 + (slot & 3) * 256, (u32x4){pk2(o[0] * inv, o[1] * inv), pk2(o[2] * inv, o[3] * inv), pk2(o[4] * inv, o[5] * inv), pk2(o[6] * inv, o[7] * inv)});
        if (h == 0) {
            const int s4 = a5 + 4;
            st64(rws, act + (s4 >> 2) * 1024 + (s4 & 3) * 256 + odd * 8, (u32x2){pk2(o[8] * inv, o[9] * inv), pk2(o[10] * inv, o[11] * inv)});
        }
    }
    ST(3);
    const unsigned fo = FL_O + (qb * 32 + head) * 4;
    if (wid >= 4) {
        WAIT_LGKM0();
        BAR();
        signal_wg(ws, lds0 + L_CNT + 12, 8, fo, 1, tag, lane);
        return;
    }
    {
        const int tok = mt * 64 + wid * 16 + (lane & 15), rq = lane >> 4;
        const unsigned off = (unsigned)(tok * D_ + nt * 80 + rq * 20) * 2u;
        f32x4 acc[5];
#pragma unroll
        for (int i = 0; i < 5; ++i) acc[i] = (f32x4){0.f, 0.f, 0.f, 0.f};
        bf16x8 aa[2], wa[2][5], ab[2], wb[2][5];
        const unsigned wbase = lds0 + lane * 16, abase = lds0 + 20480 + wid * 4096 + lane * 16;
#define RDA(dst, k) dst = ldsr128(as + (k) * 1024)
#define RDW(dst, k, ng) dst = ldsr128(wsb + (k) * 5120 + (ng) * 1024)
#define PHASE(af, wf, na, nw, hf) do { \
        _Pragma("unroll") for (int k = 0; k < 2; ++k) { \
            acc[0] = MFMA16(wf[k][0], af[k], acc[0]); SBAR(); RDA(na[k], (hf) * 2 + k); SBAR(); \
            acc[1] = MFMA16(wf[k][1], af[k], acc[1]); SBAR(); RDW(nw[k][0], (hf) * 2 + k, 0); SBAR(); \
            acc[2] = MFMA16(wf[k][2], af[k], acc[2]); SBAR(); RDW(nw[k][1], (hf) * 2 + k, 1); RDW(nw[k][2], (hf) * 2 + k, 2); SBAR(); \
            acc[3] = MFMA16(wf[k][3], af[k], acc[3]); SBAR(); RDW(nw[k][3], (hf) * 2 + k, 3); SBAR(); \
            acc[4] = MFMA16(wf[k][4], af[k], acc[4]); SBAR(); RDW(nw[k][4], (hf) * 2 + k, 4); SBAR(); \
        } } while (0)
#define P3_STEP(st) do { \
            { const unsigned so = ((st) & 3) * P3_SLOT; const unsigned as = abase + so, wsb = wbase + so; PHASE(aa, wa, ab, wb, 1); } \
            WAIT_LGKM0(); BAR(); \
            { const unsigned so = (((st) + 1) & 3) * P3_SLOT; const unsigned as = abase + so, wsb = wbase + so; PHASE(ab, wb, aa, wa, 0); } } while (0)
        WAIT_LGKM0();
        BAR();
        ST(4);
        { const unsigned as = abase, wsb = wbase;
#pragma unroll
          for (int k = 0; k < 2; ++k) { RDA(aa[k], k);
#pragma unroll
            for (int ng = 0; ng < 5; ++ng) RDW(wa[k][ng], k, ng); } }
#pragma unroll 1
        for (int st = 0; st < 2; ++st) P3_STEP(st);
        signal_wg(ws, lds0 + L_CNT + 12, 8, fo, 1, tag, lane);
#pragma unroll 1
        for (int st = 2; st < 13; ++st) P3_STEP(st);
        if (!nret) ST(6);
        u32x2 xr[5];
#pragma unroll
        for (int ng = 0; ng < 5; ++ng) xr[ng] = *(const u32x2*)(x + off + ng * 8);
#pragma unroll 1
        for (int st = 13; st < P3_NST; ++st) P3_STEP(st);
#undef P3_STEP
#undef PHASE
#undef RDA
#undef RDW
        if (!nret) ST(7);
#pragma unroll
        for (int ng = 0; ng < 5; ++ng) {
            const float r0 = bflo(xr[ng].x) + acc[ng][0], r1 = bfhi(xr[ng].x) + acc[ng][1];
            const float r2 = bflo(xr[ng].y) + acc[ng][2], r3 = bfhi(xr[ng].y) + acc[ng][3];
            *(u32x2*)(out + off + ng * 8) = (u32x2){pk2(r0, r1), pk2(r2, r3)};
        }
        if (!nret) ST(8);
    }
    if (wid == 0) ST_OUT(0);
}

extern "C" void kernel_launch(void* const* d_in, const int* in_sizes, int n_in, void* d_out, int out_size, void* d_ws, size_t ws_size, hipStream_t stream) {
    (void)hipFuncSetAttribute((const void*)mega_k, hipFuncAttributeMaxDynamicSharedMemorySize, LDS_BYTES);
    mega_k<<<256, 768, LDS_BYTES, stream>>>((const char*)d_in[0], (const char*)d_in[1], (const char*)d_in[2], (const char*)d_in[4], (const char*)d_in[5],
                                            (const char*)d_in[6], (char*)d_ws, (char*)d_out);
}
```

```cpp
#include <hip/hip_runtime.h>
#include <hip/hip_bf16.h>
#include <stdint.h>

typedef unsigned short u16;
typedef short bf16x8 __attribute__((ext_vector_type(8)));
typedef short s16x4 __attribute__((ext_vector_type(4)));
typedef float f32x16 __attribute__((ext_vector_type(16)));
typedef float f32x4 __attribute__((ext_vector_type(4)));
typedef float f32x2 __attribute__((ext_vector_type(2)));
typedef unsigned u32x4 __attribute__((ext_vector_type(4)));
typedef unsigned u32x2 __attribute__((ext_vector_type(2)));
typedef __bf16 bf16x2_t __attribute__((ext_vector_type(2)));
typedef int srd_t __attribute__((ext_vector_type(4)));
typedef short v4i16_t __attribute__((ext_vector_type(4)));
#define LAS __attribute__((address_space(3)))
#define GAS __attribute__((address_space(1)))
#define DI __device__ __forceinline__

constexpr int D_ = 640, DH = 20;
constexpr int NKS3 = 74;
constexpr unsigned FL_E = 0, FL_TMO = 128, FL_S = 1024, FL_Q = 2048, FL_H = 3072, FL_O = 4096, FL_W = 5120, FL_ST = 6144, FL_CQ = 7168, FL_CG = 7424;
constexpr unsigned WS_STAT = 16384;
constexpr unsigned WS_CSQ = 32768;
constexpr unsigned WS_CSG = WS_CSQ + 8192;
constexpr unsigned WS_QKV = 65536;
constexpr unsigned WS_ACT = WS_QKV + 480u * 16384u;
constexpr unsigned WS_WT3 = WS_ACT + 128u * NKS3 * 1024u;
constexpr unsigned WS_END = WS_WT3 + 8u * NKS3 * 5u * 1024u;
constexpr int L_QS = 131072;
constexpr int AT_PL = 4096 + 32;
constexpr int L_K = 110592, L_V = L_K + 5 * AT_PL, L_ONES = L_K + 10 * AT_PL, L_QP = L_ONES + 64, L_TAG = L_QP + 10240, L_CNT = L_TAG + 16, L_GO = L_TAG + 48,
              L_CSG = L_TAG + 64, L_QC = L_CSG, LDS_BYTES = 163840;
static_assert(L_CSG + 1024 <= LDS_BYTES && (L_CSG & 15) == 0, "lds");
constexpr int P3_SLOT = 36864, P3_NST = 19;

DI unsigned wslot(int S) { const int s3 = S < 3 ? S : S - 3; return S < 6 ? (s3 < 2 ? s3 * 32768 : L_K + 16384) : (S & 1) * 32768; }
DI unsigned xslot(int S) { const int s3 = S < 3 ? S : S - 3; return S < 6 ? (s3 < 2 ? 65536 + s3 * 16384 : L_K) : 65536 + (S & 1) * 16384; }
DI unsigned pk2(float a, float b) { f32x2 v = {a, b}; bf16x2_t r = __builtin_convertvector(v, bf16x2_t); return __builtin_bit_cast(unsigned, r); }
DI s16x4 vtr(unsigned a) { return __builtin_bit_cast(s16x4, __builtin_amdgcn_ds_read_tr16_b64_v4i16((LAS v4i16_t*)(size_t)a)); }
DI bf16x8 cat8(s16x4 a, s16x4 b) { return __builtin_shufflevector(a, b, 0, 1, 2, 3, 4, 5, 6, 7); }
DI bf16x8 ldsr128(unsigned a) { return *(const LAS bf16x8*)(size_t)a; }
DI float bflo(unsigned v) { return __uint_as_float(v << 16); }
DI float bfhi(unsigned v) { return __uint_as_float(v & 0xffff0000u); }
DI srd_t make_srd(const void* base, unsigned bytes) {
    const uint64_t v = (uint64_t)base;
    srd_t r; r[0] = (int)(unsigned)v; r[1] = (int)((unsigned)(v >> 32) & 0xffffu); r[2] = (int)bytes; r[3] = 0x00020000; return r;
}
template <int OFF, int SC> DI void bdma(unsigned voff, srd_t srd, unsigned soff, unsigned dst) {
    if (SC == 2) asm volatile("s_mov_b32 m0, %3\n\ts_nop 0\n\tbuffer_load_dwordx4 %0, %1, %2 offen offset:%4 nt lds" ::"v"(voff), "s"(srd), "s"(soff), "s"(dst), "n"(OFF) : "memory");
    else if (SC) asm volatile("s_mov_b32 m0, %3\n\ts_nop 0\n\tbuffer_load_dwordx4 %0, %1, %2 offen offset:%4 sc1 lds" ::"v"(voff), "s"(srd), "s"(soff), "s"(dst), "n"(OFF) : "memory");
    else asm volatile("s_mov_b32 m0, %3\n\ts_nop 0\n\tbuffer_load_dwordx4 %0, %1, %2 offen offset:%4 lds" ::"v"(voff), "s"(srd), "s"(soff), "s"(dst), "n"(OFF) : "memory");
}
template <int OFF, int SC> DI void bdma_m0(unsigned voff, srd_t srd, unsigned soff) {
    if (SC) asm volatile("buffer_load_dwordx4 %0, %1, %2 offen offset:%3 sc1 lds" ::"v"(voff), "s"(srd), "s"(soff), "n"(OFF) : "memory");
    else asm volatile("buffer_load_dwordx4 %0, %1, %2 offen offset:%3 lds" ::"v"(voff), "s"(srd), "s"(soff), "n"(OFF) : "memory");
}
#define WAIT_VM(n) asm volatile("s_waitcnt vmcnt(%0)" ::"n"(n) : "memory")
#define WAIT_LGKM0() asm volatile("s_waitcnt lgkmcnt(0)" ::: "memory")
#define BAR() do { asm volatile("" ::: "memory"); __builtin_amdgcn_s_barrier(); asm volatile("" ::: "memory"); } while (0)
#define SBAR() __builtin_amdgcn_sched_barrier(0)
#define PIN(x) asm volatile("" : "+v"(x))
#define MFMA32(a, b, c) __builtin_amdgcn_mfma_f32_32x32x16_bf16((a), (b), (c), 0, 0, 0)
#define MFMA16(a, b, c) __builtin_amdgcn_mfma_f32_16x16x32_bf16((a), (b), (c), 0, 0, 0)
#define RLX_AGENT __ATOMIC_RELAXED, __HIP_MEMORY_SCOPE_AGENT
typedef GAS unsigned gu32;

#define LS(i)
#define FS(i)
#define ST(i)
#define ST_DECL
#define ST_OUT(kid)

DI void st64(__amdgpu_buffer_rsrc_t rs, unsigned off, u32x2 v) { __builtin_amdgcn_raw_buffer_store_b64(v, rs, off, 0, 16); }
DI void st128(__amdgpu_buffer_rsrc_t rs, unsigned off, u32x4 v) { __builtin_amdgcn_raw_buffer_store_b128(v, rs, off, 0, 16); }
DI void signal_wg(char* ws, unsigned cnt, unsigned nwaves, unsigned off, int nf, unsigned tag, int lane) {
    WAIT_VM(0);
    unsigned old = 0;
    if (lane == 0) old = __hip_atomic_fetch_add((LAS unsigned*)(size_t)cnt, 1u, __ATOMIC_RELAXED, __HIP_MEMORY_SCOPE_WORKGROUP);
    old = __builtin_amdgcn_readfirstlane(old);
    if (old == nwaves - 1 && lane < nf) __hip_atomic_store((gu32*)(ws + off + lane * 64), tag, RLX_AGENT);
}
DI unsigned flag_load(const char* ws, unsigned off) { unsigned v; asm volatile("global_load_dword %0, %1, %2 sc1" : "=v"(v) : "v"(off), "s"(ws) : "memory"); return v; }
DI void poll1(char* ws, unsigned off, unsigned tag) {
    for (unsigned spins = 0;; ++spins) {
        unsigned v = flag_load(ws, off);
        asm volatile("s_waitcnt vmcnt(0)" : "+v"(v)::"memory");
        if (__all(v == tag)) break;
        if (spins > (1u << 17)) { __hip_atomic_store((gu32*)(ws + FL_TMO), 1u, RLX_AGENT); break; }
        __builtin_amdgcn_s_sleep(1);
    }
}

DI void poll4(char* ws, unsigned off, bool act, unsigned tag) {
    const unsigned long long dc = __ballot(!act);
    unsigned v0, v1, v2, v3, cnt = 1u << 16;
#define P4_LOOK(r) "s_waitcnt vmcnt(3)\n\tv_cmp_eq_u32 vcc, %[tag], " r "\n\ts_nop 1\n\ts_or_b64 vcc, vcc, %[dc]\n\ts_cmp_eq_u64 vcc, exec\n\ts_cbranch_scc1 2f\n\tglobal_load_dword " r ", %[off], %[ws] sc1\n\t"
    asm volatile("global_load_dword %0, %[off], %[ws] sc1\n\ts_sleep 4\n\tglobal_load_dword %1, %[off], %[ws] sc1\n\ts_sleep 4\n\t"
                 "global_load_dword %2, %[off], %[ws] sc1\n\ts_sleep 4\n\tglobal_load_dword %3, %[off], %[ws] sc1\n"
                 "1:\n\t" P4_LOOK("%0") P4_LOOK("%1") P4_LOOK("%2") P4_LOOK("%3")
                 "s_sub_u32 %[cnt], %[cnt], 1\n\ts_cbranch_scc0 1b\n"
                 "2:\n\ts_waitcnt vmcnt(0)"
                 : "=&v"(v0), "=&v"(v1), "=&v"(v2), "=&v"(v3), [cnt] "+s"(cnt)
                 : [off] "v"(off), [ws] "s"(ws), [tag] "s"(tag), [dc] "s"(dc) : "vcc", "scc", "memory");
#undef P4_LOOK
}
DI void poll2(char* ws, unsigned off, unsigned tag) {
    for (unsigned spins = 0;; ++spins) {
        u32x2 v;
        asm volatile("global_load_dwordx2 %0, %1, %2 sc1\n\ts_waitcnt vmcnt(0)" : "=&v"(v) : "v"(off), "s"(ws) : "memory");
        if (__all(v.x == tag && v.y == tag)) break;
        if (spins > (1u << 17)) { __hip_atomic_store((gu32*)(ws + FL_TMO), 1u, RLX_AGENT); break; }
        __builtin_amdgcn_s_sleep(1);
    }
}

__global__ __launch_bounds__(1024, 4) void mega_k(const char* __restrict__ x, const char* __restrict__ wqkv, const char* __restrict__ wo, const char* __restrict__ wg,
                                                 const char* __restrict__ wu, const char* __restrict__ wd, char* __restrict__ ws, char* __restrict__ out) {
    extern __shared__ __attribute__((aligned(16))) char lds[];
    ST_DECL ST(0);
    const int tid = threadIdx.x, lane = tid & 63, wid = __builtin_amdgcn_readfirstlane(tid >> 6);
    const int b = blockIdx.x, xcd = b & 7, idx = b >> 3;
    const int panel = (xcd & 1) * 8 + (idx & 7), c = (xcd >> 1) * 4 + (idx >> 3);
    const bool helper = c == 15;
    const int ct = helper ? panel : c;
    const int qb = xcd, head = idx;
    const int mt = xcd * 4 + (idx & 3), nt = idx >> 2;
    const unsigned lds0 = (unsigned)(size_t)lds;
    const bool hasQ = c < 15, hasG = ct < 14 && c != 14;
    const int nret = c == 15 ? 3 : (c == 14 ? (64 + panel < NKS3 ? 2 : 1) : 0);
    const int ret0 = c == 15 ? panel : 48 + panel;

    if (wid >= 8) {
        __builtin_amdgcn_s_setprio(3);
        const int l = wid - 8;
        const int r8 = lane >> 3, cc = lane & 7;
        const unsigned csw = (cc ^ (((r8 >> 1) & 1) << 2)) * 16;
        const unsigned vq = r8 * 3840 + csw, vg = r8 * 3456 + csw;
        const unsigned vx0 = r8 * 1280 + ((cc ^ (r8 >> 1)) * 16), vx1 = r8 * 1280 + ((cc ^ (4 + (r8 >> 1))) * 16);
        const unsigned vl = lane * 16;
        const bool isW = l < 4;
        const int l4 = l & 3;
        const srd_t sx = make_srd(x + (unsigned)(panel * 128 * 1280), 128 * 1280);
        auto issue_x = [&](int kb, unsigned dst) {
#pragma unroll
            for (int i = 0; i < 4; ++i) bdma<0, 0>((i & 1) ? vx1 : vx0, sx, (4 * l4 + i) * 10240 + kb * 128, dst + (4 * l4 + i) * 1024);
        };
        auto issue_ret = [&](int u, unsigned dst) {
            const srd_t sr = u < 54 ? make_srd(wd, 1728u * 1280u) : make_srd(wo, 640u * 1280u);
#pragma unroll 1
            for (int i = 0; i < 5; ++i) {
                const int ci = (l * 5 + i) * 64 + lane, sr_ = ci / 80, ch = ci - sr_ * 80;
                const int kg = sr_ >> 3, j = sr_ & 7;
                int row;
                if (u < 54) row = 32 * u + 16 * (kg >> 1) + 8 * (j >> 2) + 4 * (kg & 1) + (j & 3);
                else {
                    const int slot = 4 * (u - 54) + kg, a = slot / 5, ii = slot - 5 * a;
                    row = ii < 4 ? (2 * a + (ii >> 1)) * DH + (ii & 1) * 4 + (j & 3) + 8 * (j >> 2) : (2 * a + (j >> 2)) * DH + 16 + (j & 3);
                }
                bdma<0, 2>(row * 1280 + ch * 16, sr, 0, dst + (l * 5 + i) * 1024);
            }
        };
        const int np = ct < 13 ? 2 : 1;
        if (!helper) {
            const unsigned gbytes = (l4 < 2 || np == 2) ? 640u * 3456u - ct * 256 - (l4 >> 1) * 128 : 0u;
            const srd_t sg = make_srd(((l4 & 1) ? wu : wg) + ct * 256 + (l4 >> 1) * 128, gbytes);
            auto issue_g = [&](int S) {
                if (isW) {
                    const unsigned dw = lds0 + wslot(S) + l4 * 4096, so = S * 64 * 3456;
#pragma unroll
                    for (int i = 0; i < 8; ++i) bdma<0, 0>(vg, sg, so + i * 8 * 3456, dw + (i >> 2) * 16384 + (i & 3) * 1024);
                } else issue_x(S, lds0 + xslot(S));
            };
            const srd_t sw = make_srd(wqkv + c * 256 + (l4 >> 1) * 128, 640u * 3840u - c * 256 - (l4 >> 1) * 128);
            auto issue = [&](int st) {
                const unsigned sl = lds0 + (st & 3) * 32768;
                if (isW) {
#pragma unroll
                    for (int i = 0; i < 4; ++i) bdma<0, 0>(vq, sw, (st * 64 + 32 * (l4 & 1) + 8 * i) * 3840, sl + 16384 + l4 * 4096 + i * 1024);
                } else issue_x(st, sl);
            };
            issue(0);
            __builtin_amdgcn_s_sleep(2);
            issue(1); issue(2);
#pragma unroll 1
            for (int st = 0; st <= 6; ++st) { WAIT_VM(8); BAR(); LS(st); issue(st + 3); }
            WAIT_VM(8); BAR(); LS(7);
            if (hasG && !isW) { issue_g(0); WAIT_VM(8); } else { WAIT_VM(4); }
            BAR(); LS(8);
            if (hasG && !isW) { WAIT_VM(4); } else { WAIT_VM(0); }
            BAR(); LS(9);
            if (hasG && isW) issue_g(0);
            BAR(); LS(10);
            BAR(); LS(11);
            if (hasG) {
                issue_g(1);
                if (isW) { WAIT_VM(8); } else { WAIT_VM(4); }
                BAR(); LS(12);
                issue_g(2);
#pragma unroll 1
                for (int k = 1; k <= 10; ++k) {
                    if (k >= 7) { WAIT_VM(0); } else if (isW) { WAIT_VM(8); } else { WAIT_VM(4); }
                    BAR(); LS(12 + k);
                    if (k <= 5) issue_g(k + 2); else if (k == 7 || k == 8) issue_g(k + 1);
                }
            }
        } else if (hasG) {
            const int u4 = l >> 1;
            const unsigned gbytes = (u4 < 2 || np == 2) ? 640u * 3456u - ct * 256 - (u4 >> 1) * 128 : 0u;
            const srd_t sh = make_srd(((u4 & 1) ? wu : wg) + ct * 256 + (u4 >> 1) * 128, gbytes);
            auto issue_h = [&](int st) {
                const unsigned dw = lds0 + (st & 7) * 16384 + u4 * 4096 + (l & 1) * 2048, so = (st * 32 + (l & 1) * 16) * 3456;
                bdma<0, 2>(vg, sh, so, dw); bdma<0, 2>(vg, sh, so + 8 * 3456, dw + 1024);
            };
#pragma unroll 1
            for (int st = 0; st < 7; ++st) issue_h(st);
#pragma unroll 1
            for (int st = 0; st <= 20; ++st) {
                if (st <= 13) { WAIT_VM(12); } else { WAIT_VM(0); }
                BAR();
                if (st + 7 < 20) issue_h(st + 7);
            }
        }
        if (nret) {
            issue_ret(ret0, lds0);
            if (nret > 1) issue_ret(ret0 + 16, lds0 + 40960);
            if (nret > 2) issue_ret(ret0 + 32, lds0 + 81920);
            WAIT_VM(0);
            BAR();
            BAR();
        }
        if (hasG && !helper) BAR();
        BAR(); LS(23);
        ST(1);
        const unsigned tag = __builtin_amdgcn_readfirstlane(*(const volatile LAS unsigned*)(size_t)(lds0 + L_TAG));
        if (b == 0 && l == 1) {
#pragma unroll 1
            for (int i = 0; i < 4; ++i) poll1(ws, FL_S + (i * 64 + lane) * 4, tag);
            if (lane == 0) __hip_atomic_store((gu32*)(ws + FL_E), tag, RLX_AGENT);
        }
        {
            if (l == 0) {
                poll2(ws, FL_W + (lane < 37 ? lane : 0) * 8, tag);
                if (lane == 0) *(volatile LAS unsigned*)(size_t)(lds0 + L_GO) = tag;
            } else if (l == 4) {
                poll4(ws, FL_H + ((mt >> 1) * 16 + (lane & 15)) * 4, (lane & 15) < 9, tag);
                if (lane == 0) *(volatile LAS unsigned*)(size_t)(lds0 + L_GO + 4) = tag;
            } else {
                const unsigned go = lds0 + L_GO + (isW ? 0 : 4);
                for (unsigned spins = 0; *(const volatile LAS unsigned*)(size_t)go != tag && spins < (1u << 22); ++spins) __builtin_amdgcn_s_sleep(2);
            }
            ST(2);
            const srd_t sa = make_srd(ws + WS_ACT + (unsigned)((mt * 4 + l4) * NKS3 * 1024), NKS3 * 1024);
            const srd_t sw = make_srd(ws + WS_WT3 + (unsigned)(nt * NKS3 * 5 * 1024), NKS3 * 5 * 1024);
            if (isW) {
                auto issue = [&](int st) {
                    const unsigned sl = lds0 + (st & 3) * P3_SLOT, ow = st * 20480 + l4 * 5120;
                    bdma<0, 1>(vl, sw, ow, sl + l4 * 5120); bdma_m0<1024, 1>(vl, sw, ow); bdma_m0<2048, 1>(vl, sw, ow); bdma_m0<3072, 1>(vl, sw, ow);
                    bdma<0, 1>(vl, sw, ow + 4096, sl + l4 * 5120 + 4096);
                };
                issue(0); __builtin_amdgcn_s_sleep(2); issue(1); issue(2);
                ST(5);
#pragma unroll 1
                for (int st = 0; st <= P3_NST; ++st) {
                    if (st + 2 < P3_NST) { WAIT_VM(10); } else if (st + 1 < P3_NST) { WAIT_VM(5); } else { WAIT_VM(0); }
                    BAR();
                    if (st + 3 < P3_NST) issue(st + 3);
                }
            } else {
                auto issue = [&](int st) {
                    const unsigned sl = lds0 + (st & 3) * P3_SLOT;
                    bdma<0, 1>(vl, sa, st * 4096, sl + 20480 + l4 * 4096); bdma_m0<1024, 1>(vl, sa, st * 4096); bdma_m0<2048, 1>(vl, sa, st * 4096); bdma_m0<3072, 1>(vl, sa, st * 4096);
                };
                auto step = [&](int st) {
                    if (st + 2 < P3_NST) { WAIT_VM(8); } else if (st + 1 < P3_NST) { WAIT_VM(4); } else { WAIT_VM(0); }
                    BAR(); LS(24 + st);
                    if (st + 3 < P3_NST) issue(st + 3);
                };
                issue(0); __builtin_amdgcn_s_sleep(2); issue(1); issue(2);
                ST(5);
#pragma unroll 1
                for (int st = 0; st <= 2; ++st) step(st);
                const unsigned hfo = FL_H + ((mt >> 1) * 16 + (lane & 15)) * 4;
                unsigned ph = flag_load(ws, hfo);
#pragma unroll 1
                for (int st = 3; st <= 5; ++st) step(st);
                asm volatile("s_waitcnt vmcnt(12)" : "+v"(ph)::"memory");
                if (!__all((lane & 15) >= 14 || ph == tag)) poll4(ws, hfo, (lane & 15) < 14, tag);
                step(6);
                const unsigned ofo = FL_O + (qb * 32 + (lane & 31)) * 4;
                unsigned pf = flag_load(ws, ofo);
#pragma unroll 1
                for (int st = 7; st <= 9; ++st) step(st);
                asm volatile("s_waitcnt vmcnt(12)" : "+v"(pf)::"memory");
                if (!__all(pf == tag)) poll1(ws, ofo, tag);
#pragma unroll 1
                for (int st = 10; st <= P3_NST; ++st) step(st);
            }
        }
        ST(3);
        if (l == 0 || l == 4) ST_OUT(4 + (l >> 2));
        return;
    }

    const int cl = lane & 31, h = lane >> 5;
    const int g = lane >> 4, q = (lane & 15) >> 2, p = lane & 3;
    const int th = wid & 1;
    const __amdgpu_buffer_rsrc_t rws = __builtin_amdgcn_make_buffer_rsrc(ws, 0, WS_END, 0x00020000);
    const unsigned wlane = (8 * (g >> 1) + q) * 128 + (p & 1) * 8;
    const unsigned xrow = lds0 + (64 * th + cl) * 128, xsw = (cl >> 1) & 7;
    const bf16x8 ONES8 = {0x3f80, 0x3f80, 0x3f80, 0x3f80, 0x3f80, 0x3f80, 0x3f80, 0x3f80};
    float mean[2] = {0.f, 0.f}, rstd[2] = {0.f, 0.f};
    unsigned tag = 0;
    if (wid == 0) {
        if (lane >= 4 && lane < 14) *(LAS unsigned*)(size_t)(lds0 + L_CNT + (lane - 4) * 4) = 0u;
        WAIT_LGKM0();
    } else if (wid == 1) {
        const unsigned e = __hip_atomic_load((gu32*)(ws + FL_E), RLX_AGENT);
        const int tok = 8 * b + (lane >> 3);
        u32x4 v[10];
#pragma unroll
        for (int i = 0; i < 10; ++i) v[i] = *(const u32x4*)(x + (unsigned)(tok * 1280 + ((lane & 7) + 8 * i) * 16));
        tag = __builtin_amdgcn_readfirstlane(e) + 1u;
        if (lane == 0) *(LAS unsigned*)(size_t)(lds0 + L_TAG) = tag;
        float s = 0.f;
#pragma unroll
        for (int i = 0; i < 10; ++i)
#pragma unroll
            for (int j = 0; j < 4; ++j) s += bflo(v[i][j]) + bfhi(v[i][j]);
        s += __shfl_xor(s, 1); s += __shfl_xor(s, 2); s += __shfl_xor(s, 4);
        const float mu = s * (1.f / D_);
        float qq = 0.f;
#pragma unroll
        for (int i = 0; i < 10; ++i)
#pragma unroll
            for (int j = 0; j < 4; ++j) { const float d0 = bflo(v[i][j]) - mu, d1 = bfhi(v[i][j]) - mu; qq = __builtin_fmaf(d0, d0, qq); qq = __builtin_fmaf(d1, d1, qq); }
        qq += __shfl_xor(qq, 1); qq += __shfl_xor(qq, 2); qq += __shfl_xor(qq, 4);
        const float rs = 1.f / __builtin_sqrtf(qq * (1.f / D_) + 1e-5f);
        if ((lane & 7) == 0) st64(rws, WS_STAT + tok * 8, (u32x2){__float_as_uint(mu), __float_as_uint(rs)});
        WAIT_LGKM0();
    }
    auto touch = [&](int t) {
        const int cl4 = idx >> 3, pl8 = idx & 7;
        srd_t sr; unsigned off;
        if (t < 5) { sr = make_srd(x, 2048u * 1280u); off = panel * 163840 + (320 * cl4 + 64 * t + lane) * 128; }
        else if (t < 8) { const int n = (t - 5) * 64 + lane; sr = make_srd(wqkv, 640u * 3840u); off = n < 160 ? (80 * pl8 + (n >> 1)) * 3840 + c * 256 + (n & 1) * 128 : 0xffffff00u; }
        else { const int m = ((t - 8) % 3) * 64 + lane; sr = make_srd(t < 11 ? wg : wu, c < 14 ? 640u * 3456u : 0u);
               off = (m < 160 && !(c == 13 && (m & 1))) ? (80 * pl8 + (m >> 1)) * 3456 + c * 256 + (m & 1) * 128 : 0xffffff00u; }
        asm volatile("s_mov_b32 m0, %2\n\ts_nop 0\n\tbuffer_load_dword %0, %1, 0 offen sc1 lds" ::"v"(off), "s"(sr), "s"(lds0 + L_QS) : "memory");
    };
    auto stat_flag = [&]() {
        if (wid == 1) {
            WAIT_VM(0);
            if (lane == 0) { __hip_atomic_store((gu32*)(ws + FL_ST) + b, tag, RLX_AGENT); __hip_atomic_store((gu32*)(ws + FL_S) + b, tag, RLX_AGENT); }
        }
    };
    unsigned att_fo;
    {
        const int part = lane >> 3, cand = (lane >> 2) & 1, pn = lane & 3;
        const int f0 = part * 640 + head * DH + (cand ? DH - 1 : 0);
        int pp = 2 * qb - 2 + pn; if (pp < 0) pp = 0;
        att_fo = FL_Q + (pp * 16 + (lane < 24 ? (f0 >> 7) : 0)) * 4;
    }
    auto issue_att = [&](int i0, int i1) {
        const srd_t sq = make_srd(ws + WS_QKV, 480u * 16384u);
        const int kb0 = 256 * qb - 256;
        const unsigned vl = lane * 16;
#pragma unroll
        for (int i = 0; i < 5; ++i) {
            if (i >= i0 && i < i1) {
                const int pc = wid * 5 + i, pl = pc >> 2, qu = pc & 3;
                int key0 = kb0 + qu * 128; if (key0 < 0) key0 += 256;
                const int gp = (pl < 5 ? 160 : 315) + head * 5 + pl;
                bdma<0, 1>(vl, sq, gp * 16384 + key0 * 8, lds0 + L_K + pl * AT_PL + qu * 1024);
            }
        }
        if (5 >= i0 && 5 < i1) bdma<0, 1>(vl, sq, (head * 5 + (wid >> 1)) * 16384 + (256 * qb + (wid & 1) * 128) * 8, lds0 + L_QP + wid * 1024);
        if (6 >= i0 && 6 < i1 && wid < 2) bdma<0, 1>(vl, sq, (head * 5 + 4) * 16384 + (256 * qb + wid * 128) * 8, lds0 + L_QP + (8 + wid) * 1024);
    };

    if (hasQ) {
        const int blk = wid >> 1, unit = blk >> 1, fh = blk & 1;
        const unsigned wad = lds0 + 16384 + unit * 8192 + wlane + (((fh * 4 + (g & 1) * 2 + (p >> 1)) ^ (((q >> 1) & 1) << 2)) * 16);
        unsigned xad[4];
#pragma unroll
        for (int ks = 0; ks < 4; ++ks) xad[ks] = xrow + (((2 * ks + h) ^ xsw) * 16);
        f32x16 acc[2], accs;
#pragma unroll
        for (int r = 0; r < 16; ++r) { acc[0][r] = 0.f; acc[1][r] = 0.f; accs[r] = 0.f; }
        BAR();
        ST(9);
        tag = __builtin_amdgcn_readfirstlane(*(const LAS unsigned*)(size_t)(lds0 + L_TAG));
        bf16x8 xa[2][2], wa[2], xb[2][2], wb[2];
#define Q_RDK(xx, ww, so, k0, k) do { xx[k][0] = ldsr128(xad[(k0) + k] + (so)); xx[k][1] = ldsr128(xad[(k0) + k] + (so) + 4096); \
            ww[k] = cat8(vtr(wad + (so) + ((k0) + k) * 2048), vtr(wad + (so) + ((k0) + k) * 2048 + 512)); } while (0)
#define Q_PHASE(xc, wc, xn, wn, so, k0, sel) do { \
            acc[0] = MFMA32(wc[0], xc[0][0], acc[0]); SBAR(); Q_RDK(xn, wn, so, k0, 0); SBAR(); \
            acc[1] = MFMA32(wc[0], xc[0][1], acc[1]); \
            acc[0] = MFMA32(wc[1], xc[1][0], acc[0]); SBAR(); Q_RDK(xn, wn, so, k0, 1); SBAR(); \
            acc[1] = MFMA32(wc[1], xc[1][1], acc[1]); \
            if (th == (sel)) { accs = MFMA32(wc[0], ONES8, accs); accs = MFMA32(wc[1], ONES8, accs); } \
            SBAR(); } while (0)
#define Q_STEP(st) do { const unsigned so = ((st) & 3) * 32768, sn = (((st) + 1) & 3) * 32768; \
            Q_PHASE(xa, wa, xb, wb, so, 2, 0); WAIT_LGKM0(); BAR(); Q_PHASE(xb, wb, xa, wa, sn, 0, 1); } while (0)
        Q_RDK(xa, wa, 0, 0, 0); Q_RDK(xa, wa, 0, 0, 1);
        Q_STEP(0);
        stat_flag();
#pragma unroll 1
        for (int st = 1; st < 7; ++st) {
            if (wid == st - 1) touch(8 + wid);
            Q_STEP(st);
        }
        const unsigned efo = FL_ST + (panel * 16 + (lane & 15)) * 4;
        unsigned pf = flag_load(ws, efo);
        Q_STEP(7); Q_STEP(8);
        asm volatile("s_waitcnt vmcnt(0)" : "+v"(pf)::"memory");
        if (!__all(pf == tag)) poll1(ws, efo, tag);
        u32x2 sv0, sv1;
        {
            const unsigned so_ = WS_STAT + (panel * 128 + 64 * th + cl) * 8;
            asm volatile("global_load_dwordx2 %0, %2, %3 sc1\n\tglobal_load_dwordx2 %1, %2, %3 offset:256 sc1" : "=&v"(sv0), "=&v"(sv1) : "v"(so_), "s"(ws) : "memory");
        }
        Q_STEP(9);
#undef Q_STEP
#undef Q_PHASE
#undef Q_RDK
        ST(10);
        if (cl == 0) {
#pragma unroll
            for (int gg = 0; gg < 4; ++gg) *(LAS f32x4*)(size_t)(lds0 + L_QC + (th * 128 + blk * 32 + 8 * gg + 4 * h) * 4) = (f32x4){accs[4 * gg], accs[4 * gg + 1], accs[4 * gg + 2], accs[4 * gg + 3]};
        }
        WAIT_LGKM0();
        BAR();
        asm volatile("s_waitcnt vmcnt(0)" : "+v"(sv0), "+v"(sv1)::"memory");
        mean[0] = __uint_as_float(sv0.x); rstd[0] = __uint_as_float(sv0.y); mean[1] = __uint_as_float(sv1.x); rstd[1] = __uint_as_float(sv1.y);
        const int f40 = (2 * c + unit) * 16 + fh * 8 + h;
        const float sc = (2 * c + unit < 10) ? 0.22360679774997896f * 1.4426950408889634f : 1.f;
        f32x4 cs[4];
#pragma unroll
        for (int gg = 0; gg < 4; ++gg) { const unsigned ca = lds0 + L_QC + (blk * 32 + 8 * gg + 4 * h) * 4; cs[gg] = *(const LAS f32x4*)(size_t)ca + *(const LAS f32x4*)(size_t)(ca + 512); }
        const bool odd_l = cl & 1;
#pragma unroll
        for (int t = 0; t < 2; ++t) {
            const int tok2 = panel * 128 + th * 64 + t * 32 + (cl & ~1);
            const float rs = rstd[t] * sc, nm = -mean[t];
#pragma unroll
            for (int gp = 0; gp < 2; ++gp) {
                unsigned w[2][2];
#pragma unroll
                for (int gq = 0; gq < 2; ++gq) {
                    const int gg = 2 * gp + gq;
                    float v[4];
#pragma unroll
                    for (int j = 0; j < 4; ++j) v[j] = __builtin_fmaf(nm, cs[gg][j], acc[t][4 * gg + j]) * rs;
                    w[gq][0] = pk2(v[0], v[1]); w[gq][1] = pk2(v[2], v[3]);
                }
                const unsigned s0 = odd_l ? w[0][0] : w[1][0], s1 = odd_l ? w[0][1] : w[1][1];
                const unsigned r0 = __builtin_amdgcn_mov_dpp(s0, 0xB1, 0xf, 0xf, true), r1 = __builtin_amdgcn_mov_dpp(s1, 0xB1, 0xf, 0xf, true);
                const u32x4 d = odd_l ? (u32x4){r0, r1, w[1][0], w[1][1]} : (u32x4){w[0][0], w[0][1], r0, r1};
                st128(rws, WS_QKV + (unsigned)(f40 + 4 * gp + (odd_l ? 2 : 0)) * 16384u + tok2 * 8, d);
            }
        }
        if (!hasG) signal_wg(ws, lds0 + L_CNT, 8, FL_Q + (panel * 16 + c) * 4, 1, tag, lane);
    }
    ST(1);
    if (hasG) {
        const int fh = (wid >> 1) & 1, pr = wid >> 2;
        const bool act = (ct < 13) || pr == 0;
        const unsigned wad = lds0 + pr * 8192 + wlane + (((fh * 4 + (g & 1) * 2 + (p >> 1)) ^ (((q >> 1) & 1) << 2)) * 16);
        BAR();
        ST(11);
        if (!helper) {
            unsigned xad[4];
#pragma unroll
            for (int ks = 0; ks < 4; ++ks) xad[ks] = xrow + (((2 * ks + h) ^ xsw) * 16);
            f32x16 acc[2][2];
#pragma unroll
            for (int r = 0; r < 16; ++r) { acc[0][0][r] = 0.f; acc[0][1][r] = 0.f; acc[1][0][r] = 0.f; acc[1][1][r] = 0.f; }
            bf16x8 xa[2], wa[2], xb[2], wb[2];
#define G_PHASE(xf, wf, nx, nw, wa_, xa_) do { \
            acc[0][0] = MFMA32(wf[0], xf[0], acc[0][0]); SBAR(); nx[0] = ldsr128(xa_); nw[0] = cat8(vtr(wa_), vtr((wa_) + 512)); SBAR(); \
            acc[1][0] = MFMA32(wf[0], xf[1], acc[1][0]); SBAR(); nx[1] = ldsr128((xa_) + 4096); SBAR(); \
            acc[0][1] = MFMA32(wf[1], xf[0], acc[0][1]); SBAR(); nw[1] = cat8(vtr((wa_) + 4096), vtr((wa_) + 4096 + 512)); SBAR(); \
            acc[1][1] = MFMA32(wf[1], xf[1], acc[1][1]); SBAR(); } while (0)
#define G_STEP2(S) do { \
            const unsigned w0 = wad + wslot(S), x0 = xslot(S), wn = wad + wslot((S) + 1), xn = xslot((S) + 1); \
            if (act) { G_PHASE(xa, wa, xb, wb, w0 + 2048, xad[1] + x0); G_PHASE(xb, wb, xa, wa, w0 + 16384, xad[2] + x0); G_PHASE(xa, wa, xb, wb, w0 + 16384 + 2048, xad[3] + x0); } \
            WAIT_LGKM0(); BAR(); \
            if (act) G_PHASE(xb, wb, xa, wa, wn, xad[0] + xn); } while (0)
            xa[0] = ldsr128(xad[0] + 65536); xa[1] = ldsr128(xad[0] + 65536 + 4096);
            wa[0] = cat8(vtr(wad), vtr(wad + 512)); wa[1] = cat8(vtr(wad + 4096), vtr(wad + 4096 + 512));
#pragma unroll 1
            for (int s2 = 0; s2 < 3; ++s2) G_STEP2(s2);
            signal_wg(ws, lds0 + L_CNT, 8, FL_Q + (panel * 16 + c) * 4, 1, tag, lane);
            G_STEP2(3);
            const unsigned gfo = lane < 24 ? att_fo : FL_CG + c * 4;
            unsigned pf = flag_load(ws, gfo);
#pragma unroll 1
            for (int s2 = 4; s2 < 6; ++s2) G_STEP2(s2);
            asm volatile("s_waitcnt vmcnt(0)" : "+v"(pf)::"memory");
            const bool att_ok = __all(pf == tag);
            if (att_ok) { if (wid == 2) bdma<0, 1>(lane * 16, make_srd(ws, WS_QKV), WS_CSG + c * 1024, lds0 + L_CSG); issue_att(0, 2); }
            G_STEP2(6);
            if (att_ok) issue_att(2, 4);
            G_STEP2(7);
            if (att_ok) { WAIT_VM(4); issue_att(4, 7); }
            G_STEP2(8);
            G_STEP2(9);
            ST(12);
#undef G_STEP2
#undef G_PHASE
            if (!att_ok) {
                poll1(ws, gfo, tag);
                if (wid == 2) bdma<0, 1>(lane * 16, make_srd(ws, WS_QKV), WS_CSG + c * 1024, lds0 + L_CSG);
                issue_att(0, 7);
                WAIT_VM(0);
            }
            if (tid < 2) *(LAS u32x2*)(size_t)(lds0 + L_ONES + tid * 8) = (u32x2){0x00003f80u, 0u};
            if (!att_ok && lane == 0) *(volatile LAS unsigned*)(size_t)(lds0 + L_CNT + 24) = 1u;
            WAIT_LGKM0();
            BAR();
            if (act) {
                const int ks3 = (2 * c + pr) * 2 + fh;
                const unsigned csa = lds0 + L_CSG + (pr * 128 + fh * 32) * 4;
#pragma unroll
                for (int t = 0; t < 2; ++t) {
                    const int tok = panel * 128 + th * 64 + t * 32 + cl;
                    const float rs2 = rstd[t] * rstd[t], nm = -mean[t];
#pragma unroll
                    for (int gp = 0; gp < 2; ++gp) {
                        float hv[8];
#pragma unroll
                        for (int gq = 0; gq < 2; ++gq) {
                            const int gg = 2 * gp + gq;
                            const unsigned ca = csa + (8 * gg + 4 * h) * 4;
                            const f32x4 cg = *(const LAS f32x4*)(size_t)(ca), cu = *(const LAS f32x4*)(size_t)(ca + 256);
#pragma unroll
                            for (int j = 0; j < 4; ++j)
                                hv[4 * gq + j] = __builtin_fmaf(nm, cg[j], acc[t][0][4 * gg + j]) * __builtin_fmaf(nm, cu[j], acc[t][1][4 * gg + j]) * rs2;
                        }
                        st128(rws, WS_ACT + (unsigned)((tok >> 4) * NKS3 + ks3) * 1024u + ((tok & 15) + 16 * (2 * gp + h)) * 16,
                              (u32x4){pk2(hv[0], hv[1]), pk2(hv[2], hv[3]), pk2(hv[4], hv[5]), pk2(hv[6], hv[7])});
                    }
                }
                WAIT_VM(4);
            } else WAIT_VM(0);
            BAR();
        } else {
            tag = __builtin_amdgcn_readfirstlane(*(const LAS unsigned*)(size_t)(lds0 + L_TAG));
            stat_flag();
            WAIT_VM(0);
            f32x16 acc;
#pragma unroll
            for (int r = 0; r < 16; ++r) acc[r] = 0.f;
#pragma unroll 1
            for (int st = 0; st < 20; ++st) {
                const unsigned a = wad + (st & 7) * 16384 + th * 4096;
                const bf16x8 w0 = cat8(vtr(a), vtr(a + 512)), w1 = cat8(vtr(a + 2048), vtr(a + 2048 + 512));
                acc = MFMA32(w0, ONES8, acc); acc = MFMA32(w1, ONES8, acc);
                WAIT_LGKM0();
                BAR();
            }
            ST(12);
            if (cl == 0) {
#pragma unroll
                for (int gg = 0; gg < 4; ++gg)
                    st128(rws, WS_CSG + (ct * 256 + pr * 128 + th * 64 + fh * 32 + 8 * gg + 4 * h) * 4, __builtin_bit_cast(u32x4, (f32x4){acc[4 * gg], acc[4 * gg + 1], acc[4 * gg + 2], acc[4 * gg + 3]}));
            }
            signal_wg(ws, lds0 + L_CNT + 4, 8, FL_CG + ct * 4, 1, tag, lane);
        }
    }
    if (nret) {
        if (!hasQ && !hasG) { stat_flag(); WAIT_VM(0); }
        ST(6);
        BAR();
        ST(7);
        tag = __builtin_amdgcn_readfirstlane(*(const LAS unsigned*)(size_t)(lds0 + L_TAG));
#pragma unroll 1
        for (int i = 0; i < nret; ++i) {
            const int u = ret0 + 16 * i;
            const unsigned ra = lds0 + i * 40960 + (8 * g + q) * 1280 + 160 * wid + 40 * p;
#pragma unroll
            for (int ng = 0; ng < 5; ++ng) {
                const bf16x8 f = cat8(vtr(ra + 8 * ng), vtr(ra + 8 * ng + 4 * 1280));
                st128(rws, WS_WT3 + (unsigned)((wid * NKS3 + u) * 5 + ng) * 1024u + lane * 16, __builtin_bit_cast(u32x4, f));
            }
        }
        WAIT_LGKM0();
        BAR();
        signal_wg(ws, lds0 + L_CNT + 8, 8, FL_W + ret0 * 4, nret, tag, lane);
        ST(8);
        poll1(ws, att_fo, tag);
        issue_att(0, 7);
        if (tid < 2) *(LAS u32x2*)(size_t)(lds0 + L_ONES + tid * 8) = (u32x2){0x00003f80u, 0u};
        WAIT_VM(0); WAIT_LGKM0();
        BAR();
    }
    ST(2);
    {
        bf16x8 qf0, qf1;
        {
            const unsigned qp = lds0 + L_QP + (32 * wid + cl) * 8;
            const u32x2 a = *(const LAS u32x2*)(size_t)(qp + (2 * h) * 2048), bq = *(const LAS u32x2*)(size_t)(qp + (2 * h + 1) * 2048), e = *(const LAS u32x2*)(size_t)(qp + 4 * 2048);
            qf0 = __builtin_bit_cast(bf16x8, (u32x4){a.x, a.y, bq.x, bq.y});
            qf1 = __builtin_bit_cast(bf16x8, (u32x4){h ? 0u : e.x, h ? 0u : e.y, 0u, 0u});
        }
        const unsigned kp = lds0 + L_K + (2 * h) * AT_PL + (32 * wid + cl) * 8, kp4 = lds0 + L_K + 4 * AT_PL + (32 * wid + cl) * 8;
        const float NEG = -__builtin_inff();
        const int ch = cl - 4 * h;
        const int jmin = qb == 0 ? 8 - wid : 0;
        auto stile = [&](int j, const f32x16& cin) -> f32x16 {
            const u32x2 a = *(const LAS u32x2*)(size_t)(kp + j * 256), bq = *(const LAS u32x2*)(size_t)(kp + AT_PL + j * 256), e = *(const LAS u32x2*)(size_t)(kp4 + j * 256);
            const f32x16 t = MFMA32(__builtin_bit_cast(bf16x8, (u32x4){a.x, a.y, bq.x, bq.y}), qf0, cin);
            return MFMA32(__builtin_bit_cast(bf16x8, (u32x4){e.x, e.y, e.x, e.y}), qf1, t);
        };
        auto cfill = [&](float v, int kind) -> f32x16 {
            f32x16 r_;
#pragma unroll
            for (int r = 0; r < 16; ++r) { const int rr = (r & 3) + 8 * (r >> 2); r_[r] = kind == 0 ? v : ((rr > ch) == (kind == 1) ? v : NEG); }
            return r_;
        };
        auto tmax = [&](const f32x16& t, float m_) -> float {
#pragma unroll
            for (int r = 0; r < 16; r += 2) m_ = __builtin_fmaxf(__builtin_fmaxf(m_, t[r]), t[r + 1]);
            return m_;
        };
        float m;
        {
            f32x16 cur = stile(8, cfill(0.f, 2));
            const f32x16 z = cfill(0.f, 0);
            m = NEG;
            if (jmin == 0) { f32x16 nx = stile(0, cfill(0.f, 1)); m = tmax(cur, m); cur = nx; }
#pragma unroll 1
            for (int j = jmin > 1 ? jmin : 1; j < 5; ++j) { f32x16 nx = stile(j, z); m = tmax(cur, m); cur = nx; }
            if (hasG && !helper) signal_wg(ws, lds0 + L_CNT + 4, 8, FL_H + (panel * 16 + c) * 4, 1, tag, lane);
#pragma unroll 1
            for (int j = jmin > 5 ? jmin : 5; j < 8; ++j) { f32x16 nx = stile(j, z); m = tmax(cur, m); cur = nx; }
            m = tmax(cur, m);
        }
        { auto rr = __builtin_amdgcn_permlane32_swap(__float_as_uint(m), __float_as_uint(m), false, false);
          m = __builtin_fmaxf(__uint_as_float(rr[0]), __uint_as_float(rr[1])); }
        ST(13);
        ST(5);
        const int dq = 4 * (g & 1) + p;
        const bool real = dq < 5;
        const unsigned vbase = real ? lds0 + L_V + dq * AT_PL + (32 * wid + 4 * h + q) * 8 : lds0 + L_ONES + (dq == 5 ? 0 : 8);
        const unsigned vstep = real ? 64u : 0u;
        f32x16 o = cfill(0.f, 0);
        auto pv = [&](int j, f32x16 t) {
            const unsigned va = vbase + vstep * (4 * j);
            const bf16x8 v0 = cat8(vtr(va), vtr(va + vstep)), v1 = cat8(vtr(va + 2 * vstep), vtr(va + 3 * vstep));
#pragma unroll
            for (int r = 0; r < 16; ++r) t[r] = __builtin_amdgcn_exp2f(t[r]);
            u32x4 p0, p1;
#pragma unroll
            for (int i = 0; i < 4; ++i) { p0[i] = pk2(t[2 * i], t[2 * i + 1]); p1[i] = pk2(t[8 + 2 * i], t[8 + 2 * i + 1]); }
            o = MFMA32(v0, __builtin_bit_cast(bf16x8, p0), o);
            o = MFMA32(v1, __builtin_bit_cast(bf16x8, p1), o);
        };
        {
            int jc = 8;
            f32x16 cur = stile(8, cfill(-m, 2));
            const f32x16 nm = cfill(-m, 0);
            if (jmin == 0) { f32x16 nx = stile(0, cfill(-m, 1)); pv(8, cur); cur = nx; jc = 0; }
#pragma unroll 1
            for (int j = jmin > 1 ? jmin : 1; j < 8; ++j) { f32x16 nx = stile(j, nm); pv(jc, cur); cur = nx; jc = j; }
            pv(jc, cur);
        }
        float l; { auto rr = __builtin_amdgcn_permlane32_swap(__float_as_uint(o[8]), __float_as_uint(o[8]), false, false); l = h ? o[8] : __uint_as_float(rr[1]); }
        const float inv = __builtin_amdgcn_rcpf(l);
        const int tok = 256 * qb + 32 * wid + cl;
        const int a5 = (head >> 1) * 5, odd = head & 1;
        const unsigned act = WS_ACT + (unsigned)((tok >> 4) * NKS3 + 54) * 1024u + (tok & 15) * 16;
        const int slot = a5 + odd * 2 + h;
        st128(rws, act + (slot >> 2) * 1024 + (slot & 3) * 256, (u32x4){pk2(o[0] * inv, o[1] * inv), pk2(o[2] * inv, o[3] * inv), pk2(o[4] * inv, o[5] * inv), pk2(o[6] * inv, o[7] * inv)});
        if (h == 0) {
            const int s4 = a5 + 4;
            st64(rws, act + (s4 >> 2) * 1024 + (s4 & 3) * 256 + odd * 8, (u32x2){pk2(o[8] * inv, o[9] * inv), pk2(o[10] * inv, o[11] * inv)});
        }
    }
    ST(3);
    const unsigned fo = FL_O + (qb * 32 + head) * 4;
    if (wid >= 4) {
        WAIT_LGKM0();
        BAR();
        signal_wg(ws, lds0 + L_CNT + 12, 8, fo, 1, tag, lane);
        return;
    }
    {
        const int tok = mt * 64 + wid * 16 + (lane & 15), rq = lane >> 4;
        const unsigned off = (unsigned)(tok * D_ + nt * 80 + rq * 20) * 2u;
        f32x4 acc[5];
#pragma unroll
        for (int i = 0; i < 5; ++i) acc[i] = (f32x4){0.f, 0.f, 0.f, 0.f};
        bf16x8 aa[2], wa[2][5], ab[2], wb[2][5];
        const unsigned wbase = lds0 + lane * 16, abase = lds0 + 20480 + wid * 4096 + lane * 16;
#define RDA(dst, k) dst = ldsr128(as + (k) * 1024)
#define RDW(dst, k, ng) dst = ldsr128(wsb + (k) * 5120 + (ng) * 1024)
#define PHASE(af, wf, na, nw, hf) do { \
        _Pragma("unroll") for (int k = 0; k < 2; ++k) { \
            acc[0] = MFMA16(wf[k][0], af[k], acc[0]); SBAR(); RDA(na[k], (hf) * 2 + k); SBAR(); \
            acc[1] = MFMA16(wf[k][1], af[k], acc[1]); SBAR(); RDW(nw[k][0], (hf) * 2 + k, 0); SBAR(); \
            acc[2] = MFMA16(wf[k][2], af[k], acc[2]); SBAR(); RDW(nw[k][1], (hf) * 2 + k, 1); RDW(nw[k][2], (hf) * 2 + k, 2); SBAR(); \
            acc[3] = MFMA16(wf[k][3], af[k], acc[3]); SBAR(); RDW(nw[k][3], (hf) * 2 + k, 3); SBAR(); \
            acc[4] = MFMA16(wf[k][4], af[k], acc[4]); SBAR(); RDW(nw[k][4], (hf) * 2 + k, 4); SBAR(); \
        } } while (0)
#define P3_STEP(st) do { \
            { const unsigned so = ((st) & 3) * P3_SLOT; const unsigned as = abase + so, wsb = wbase + so; PHASE(aa, wa, ab, wb, 1); } \
            WAIT_LGKM0(); BAR(); \
            { const unsigned so = (((st) + 1) & 3) * P3_SLOT; const unsigned as = abase + so, wsb = wbase + so; PHASE(ab, wb, aa, wa, 0); } } while (0)
        WAIT_LGKM0();
        BAR();
        ST(4);
        { const unsigned as = abase, wsb = wbase;
#pragma unroll
          for (int k = 0; k < 2; ++k) { RDA(aa[k], k);
#pragma unroll
            for (int ng = 0; ng < 5; ++ng) RDW(wa[k][ng], k, ng); } }
#pragma unroll 1
        for (int st = 0; st < 2; ++st) P3_STEP(st);
        signal_wg(ws, lds0 + L_CNT + 12, 8, fo, 1, tag, lane);
#pragma unroll 1
        for (int st = 2; st < 13; ++st) P3_STEP(st);
        if (!nret) ST(6);
        u32x2 xr[5];
#pragma unroll
        for (int ng = 0; ng < 5; ++ng) xr[ng] = *(const u32x2*)(x + off + ng * 8);
#pragma unroll 1
        for (int st = 13; st < P3_NST; ++st) P3_STEP(st);
#undef P3_STEP
#undef PHASE
#undef RDA
#undef RDW
        if (!nret) ST(7);
#pragma unroll
        for (int ng = 0; ng < 5; ++ng) {
            const float r0 = bflo(xr[ng].x) + acc[ng][0], r1 = bfhi(xr[ng].x) + acc[ng][1];
            const float r2 = bflo(xr[ng].y) + acc[ng][2], r3 = bfhi(xr[ng].y) + acc[ng][3];
            *(u32x2*)(out + off + ng * 8) = (u32x2){pk2(r0, r1), pk2(r2, r3)};
        }
        if (!nret) ST(8);
    }
    if (wid == 0) ST_OUT(0);
}

extern "C" void kernel_launch(void* const* d_in, const int* in_sizes, int n_in, void* d_out, int out_size, void* d_ws, size_t ws_size, hipStream_t stream) {
    (void)hipFuncSetAttribute((const void*)mega_k, hipFuncAttributeMaxDynamicSharedMemorySize, LDS_BYTES);
    mega_k<<<256, 1024, LDS_BYTES, stream>>>((const char*)d_in[0], (const char*)d_in[1], (const char*)d_in[2], (const char*)d_in[4], (const char*)d_in[5],
                                            (const char*)d_in[6], (char*)d_ws, (char*)d_out);
}
```
